```python
import torch

def _softmax(x, axis=-1):
    return torch.softmax(x, dim=axis)
def _gelu(x):
    return torch.nn.functional.gelu(x, approximate="tanh")
def _silu(x):
    return torch.nn.functional.silu(x)
def _sigmoid(x):
    return torch.sigmoid(x)
def _layernorm(x, axis=-1, eps=1e-5):
    m = x.mean(dim=axis, keepdim=True)
    v = x.var(dim=axis, keepdim=True, unbiased=False)
    return (x - m) / torch.sqrt(v + eps)
def _scatter_add(tgt, idx, src, axis=0):
    return tgt.clone().index_add_(axis, idx, src)
def _segment_sum(src, seg, n):
    out = torch.zeros((n,) + src.shape[1:], dtype=src.dtype)
    return out.index_add_(0, seg, src)
def _pad(x, pw):
    flat = tuple(v for pair in reversed(pw) for v in pair)
    return torch.nn.functional.pad(x, flat)

kernel_name = 'parallel_sum__gqa_block_cached__glu_gated_swiglu__decode'

def setup_inputs():

    _w = (lambda a: (lambda t: t.to(torch.bfloat16) if t.is_floating_point() else t)(torch.as_tensor(a)))
    def _A_setup():
        B, Sq, D = 1, 1, 640
        Hq, Hkv, Sk, Dh = 32, 32, 8192, 128
        assert Hq % Hkv == 0, (Hq, Hkv)
        return {
            "x": _w((lambda *s: torch.randn(*s, dtype=torch.bfloat16) * 0.05)(B, Sq, D)),
            "k_cache": _w((lambda *s: torch.randn(*s, dtype=torch.bfloat16) * 1.6)(B, Hkv, Sk, Dh)),
            "v_cache": _w((lambda *s: torch.randn(*s, dtype=torch.bfloat16) * 0.8)(B, Hkv, Sk, Dh)),
            "wq": _w((lambda *s: torch.randn(*s, dtype=torch.bfloat16) * 1.6)(D, Hq * Dh) * (D ** -0.5)),
            "wo": _w((lambda *s: torch.randn(*s, dtype=torch.bfloat16) * 0.8)(Hq * Dh, D) * ((Hq * Dh) ** -0.5)),
        }
    _A_in = _A_setup()

    def _B_setup():
        B, S, D, F = 1, 1, 640, 1728
        return {
            "x": _w((lambda *s: torch.randn(*s, dtype=torch.bfloat16) * 0.05)(B, S, D)),
            "w1": _w((lambda *s: torch.randn(*s, dtype=torch.bfloat16) * 0.4)(D, F) * (2.0 / D) ** 0.5),
            "w3": _w((lambda *s: torch.randn(*s, dtype=torch.bfloat16) * 0.4)(D, F) * (2.0 / D) ** 0.5),
            "w2": _w((lambda *s: torch.randn(*s, dtype=torch.bfloat16) * 0.4)(F, D) * (2.0 / F) ** 0.5),
        }
    _B_in = _B_setup()

    return {
        "A_x": _A_in["x"],
        "A_k_cache": _A_in["k_cache"],
        "A_v_cache": _A_in["v_cache"],
        "A_wq": _A_in["wq"],
        "A_wo": _A_in["wo"],
        "B_x": _B_in["x"],
        "B_w1": _B_in["w1"],
        "B_w3": _B_in["w3"],
        "B_w2": _B_in["w2"],
    }

def reference(A_x, A_k_cache, A_v_cache, A_wq, A_wo, B_x, B_w1, B_w3, B_w2):
    def _A_ref(x, k_cache, v_cache, wq, wo):
        B, Sq, D = x.shape
        Hq, Dh = 32, 128
        Hkv = k_cache.shape[1]
        rep = Hq // Hkv
        q = (lambda x, *d: x.permute(*d))((x @ wq).reshape(B, Sq, Hq, Dh), 0, 2, 1, 3)
        k = (lambda x, n, axis: torch.repeat_interleave(x, n, dim=axis))(k_cache, rep, axis=1)
        v = (lambda x, n, axis: torch.repeat_interleave(x, n, dim=axis))(v_cache, rep, axis=1)
        scores = torch.einsum("bhqd,bhkd->bhqk", q, k) * (Dh ** -0.5)
        p = _softmax(scores, axis=-1)
        o = torch.einsum("bhqk,bhkd->bhqd", p, v)
        return (lambda x, *d: x.permute(*d))(o, 0, 2, 1, 3).reshape(B, Sq, Hq * Dh) @ wo

    def _B_ref(x, w1, w3, w2):
        gate = torch.einsum("bsd,df->bsf", x, w1)
        up = torch.einsum("bsd,df->bsf", x, w3)
        h = _silu(gate) * up
        out = torch.einsum("bsf,fd->bsd", h, w2)
        return out

    x = A_x
    xn = _layernorm(x, axis=-1)
    return x + _A_ref(xn, A_k_cache, A_v_cache, A_wq, A_wo) + _B_ref(xn, B_w1, B_w3, B_w2)
```

```cpp
#include <hip/hip_runtime.h>
#include <hip/hip_bf16.h>
#include <stdint.h>

typedef unsigned u32x4 __attribute__((ext_vector_type(4)));
typedef __bf16 bf16x2 __attribute__((ext_vector_type(2)));

constexpr int D = 640, H = 32, S = 8192, DH = 128, F = 1728, HD = H * DH;
constexpr int NSPLIT = 8, KPS = S / NSPLIT;

__device__ __forceinline__ float bf2f(unsigned short u) { return __uint_as_float((unsigned)u << 16); }
__device__ __forceinline__ float lo(unsigned u) { return __uint_as_float(u << 16); }
__device__ __forceinline__ float hi(unsigned u) { return __uint_as_float(u & 0xffff0000u); }
__device__ __forceinline__ unsigned short f2bf(float f) { __hip_bfloat16 b = __float2bfloat16(f); return *(unsigned short*)&b; }
__device__ __forceinline__ float rbf(float f) { return bf2f(f2bf(f)); }

__device__ void layernorm(const unsigned short* x, float* xn, float* red) {
  const int tid = threadIdx.x;
  float s = 0.f;
  for (int i = tid; i < D; i += blockDim.x) s += bf2f(x[i]);
  for (int o = 32; o; o >>= 1) s += __shfl_xor(s, o);
  if ((tid & 63) == 0) red[tid >> 6] = s;
  __syncthreads();
  float t = 0.f; for (int w = 0; w < (int)blockDim.x / 64; ++w) t += red[w];
  const float mean = t / D;
  __syncthreads();
  float v = 0.f;
  for (int i = tid; i < D; i += blockDim.x) { float d = bf2f(x[i]) - mean; v += d * d; }
  for (int o = 32; o; o >>= 1) v += __shfl_xor(v, o);
  if ((tid & 63) == 0) red[tid >> 6] = v;
  __syncthreads();
  t = 0.f; for (int w = 0; w < (int)blockDim.x / 64; ++w) t += red[w];
  const float var = t / D;
  const float mb = rbf(mean), sd = rbf(sqrtf(rbf(rbf(var) + 1e-5f)));
  for (int i = tid; i < D; i += blockDim.x) xn[i] = rbf(rbf(bf2f(x[i]) - mb) / sd);
  __syncthreads();
}

__global__ __launch_bounds__(256) void k_proj(const unsigned short* x, const unsigned short* wq, const unsigned short* w1, const unsigned short* w3, float* q, float* h) {
  __shared__ float xn[D]; __shared__ float red[4]; __shared__ float part[2][4][64];
  layernorm(x, xn, red);
  const int col = blockIdx.x * 64 + (threadIdx.x & 63), ks = threadIdx.x >> 6;
  if (blockIdx.x < HD / 64) {
    float a = 0.f;
    for (int k = ks; k < D; k += 4) a += xn[k] * bf2f(wq[(size_t)k * HD + col]);
    part[0][ks][threadIdx.x & 63] = a;
    __syncthreads();
    if (ks == 0) { int l = threadIdx.x; q[col] = rbf(part[0][0][l] + part[0][1][l] + part[0][2][l] + part[0][3][l]); }
  } else {
    const int c = col - HD;
    float a = 0.f, b = 0.f;
    for (int k = ks; k < D; k += 4) { a += xn[k] * bf2f(w1[(size_t)k * F + c]); b += xn[k] * bf2f(w3[(size_t)k * F + c]); }
    part[0][ks][threadIdx.x & 63] = a; part[1][ks][threadIdx.x & 63] = b;
    __syncthreads();
    if (ks == 0) { int l = threadIdx.x;
      float g = rbf(part[0][0][l] + part[0][1][l] + part[0][2][l] + part[0][3][l]);
      float u = rbf(part[1][0][l] + part[1][1][l] + part[1][2][l] + part[1][3][l]);
      h[c] = rbf(rbf(g / (1.f + __expf(-g))) * u); }
  }
}

__global__ __launch_bounds__(256) void k_attn(const unsigned short* K, const unsigned short* V, const float* q, float* pm, float* pl, float* po) {
  __shared__ float sc[KPS]; __shared__ float red[4]; __shared__ float oacc[16][DH];
  const int h = blockIdx.x / NSPLIT, s = blockIdx.x % NSPLIT, tid = threadIdx.x, c = tid & 15, r = tid >> 4;
  float qv[8];
  for (int j = 0; j < 8; ++j) qv[j] = q[h * DH + 8 * c + j];
  const unsigned short* kb = K + ((size_t)h * S + (size_t)s * KPS) * DH;
  const unsigned short* vb = V + ((size_t)h * S + (size_t)s * KPS) * DH;
  for (int k = r; k < KPS; k += 16) {
    u32x4 kv = __builtin_nontemporal_load((const u32x4*)(kb + (size_t)k * DH + 8 * c));
    float a = 0.f;
    for (int j = 0; j < 4; ++j) a += lo(kv[j]) * qv[2 * j] + hi(kv[j]) * qv[2 * j + 1];
    for (int o = 8; o; o >>= 1) a += __shfl_xor(a, o);
    if (c == 0) sc[k] = a * 0.08838834764831845f;
  }
  __syncthreads();
  float m = -INFINITY;
  for (int k = tid; k < KPS; k += 256) m = fmaxf(m, sc[k]);
  for (int o = 32; o; o >>= 1) m = fmaxf(m, __shfl_xor(m, o));
  if ((tid & 63) == 0) red[tid >> 6] = m;
  __syncthreads();
  m = fmaxf(fmaxf(red[0], red[1]), fmaxf(red[2], red[3]));
  __syncthreads();
  float l = 0.f;
  for (int k = tid; k < KPS; k += 256) { float p = __expf(sc[k] - m); sc[k] = p; l += p; }
  for (int o = 32; o; o >>= 1) l += __shfl_xor(l, o);
  if ((tid & 63) == 0) red[tid >> 6] = l;
  __syncthreads();
  l = red[0] + red[1] + red[2] + red[3];
  float acc[8] = {0, 0, 0, 0, 0, 0, 0, 0};
  for (int k = r; k < KPS; k += 16) {
    u32x4 vv = __builtin_nontemporal_load((const u32x4*)(vb + (size_t)k * DH + 8 * c));
    const float p = sc[k];
    for (int j = 0; j < 4; ++j) { acc[2 * j] += p * lo(vv[j]); acc[2 * j + 1] += p * hi(vv[j]); }
  }
  for (int j = 0; j < 8; ++j) oacc[r][8 * c + j] = acc[j];
  __syncthreads();
  if (tid < DH) { float o = 0.f; for (int rr = 0; rr < 16; ++rr) o += oacc[rr][tid]; po[(size_t)blockIdx.x * DH + tid] = o; }
  if (tid == 0) { pm[blockIdx.x] = m; pl[blockIdx.x] = l; }
}

__global__ __launch_bounds__(256) void k_out(const unsigned short* x, const unsigned short* wo, const unsigned short* w2, const float* h, const float* pm, const float* pl, const float* po, unsigned short* out) {
  __shared__ float o[HD]; __shared__ float part[2][16][16];
  const int tid = threadIdx.x;
  for (int i = tid; i < HD; i += 256) {
    const int hh = i / DH, d = i % DH;
    float M = -INFINITY; for (int s = 0; s < NSPLIT; ++s) M = fmaxf(M, pm[hh * NSPLIT + s]);
    float L = 0.f, a = 0.f;
    for (int s = 0; s < NSPLIT; ++s) { float w = __expf(pm[hh * NSPLIT + s] - M); L += w * pl[hh * NSPLIT + s]; a += w * po[(size_t)(hh * NSPLIT + s) * DH + d]; }
    o[i] = rbf(a / L);
  }
  __syncthreads();
  const int col = blockIdx.x * 16 + (tid & 15), ks = tid >> 4;
  float a = 0.f, b = 0.f;
  for (int k = ks; k < HD; k += 16) a += o[k] * bf2f(wo[(size_t)k * D + col]);
  for (int k = ks; k < F; k += 16) b += h[k] * bf2f(w2[(size_t)k * D + col]);
  part[0][ks][tid & 15] = a; part[1][ks][tid & 15] = b;
  __syncthreads();
  if (tid < 16) {
    float A = 0.f, B = 0.f;
    for (int k = 0; k < 16; ++k) { A += part[0][k][tid]; B += part[1][k][tid]; }
    out[col] = f2bf(rbf(bf2f(x[col]) + rbf(A)) + rbf(B));
  }
}

extern "C" void kernel_launch(void* const* d_in, const int* in_sizes, int n_in, void* d_out, int out_size, void* d_ws, size_t ws_size, hipStream_t stream) {
  const unsigned short* x = (const unsigned short*)d_in[0];
  const unsigned short* K = (const unsigned short*)d_in[1];
  const unsigned short* V = (const unsigned short*)d_in[2];
  const unsigned short* wq = (const unsigned short*)d_in[3];
  const unsigned short* wo = (const unsigned short*)d_in[4];
  const unsigned short* w1 = (const unsigned short*)d_in[6];
  const unsigned short* w3 = (const unsigned short*)d_in[7];
  const unsigned short* w2 = (const unsigned short*)d_in[8];
  float* ws = (float*)d_ws;
  float* q = ws; float* h = q + HD; float* pm = h + 2048; float* pl = pm + 256; float* po = pl + 256;
  k_proj<<<HD / 64 + F / 64, 256, 0, stream>>>(x, wq, w1, w3, q, h);
  k_attn<<<H * NSPLIT, 256, 0, stream>>>(K, V, q, pm, pl, po);
  k_out<<<D / 16, 256, 0, stream>>>(x, wo, w2, h, pm, pl, po, (unsigned short*)d_out);
}
```

```cpp
#include <hip/hip_runtime.h>
#include <stdint.h>

namespace fused {

typedef unsigned u32;
typedef unsigned long long u64;
typedef u32 u32x2 __attribute__((ext_vector_type(2)));
typedef u32 u32x4 __attribute__((ext_vector_type(4)));
typedef float f32x2 __attribute__((ext_vector_type(2)));
typedef float f32x4 __attribute__((ext_vector_type(4)));
typedef short s16x4 __attribute__((ext_vector_type(4)));
typedef short s16x8 __attribute__((ext_vector_type(8)));
typedef __bf16 bf16x2 __attribute__((ext_vector_type(2)));
typedef __bf16 bf16x8 __attribute__((ext_vector_type(8)));
#define LAS __attribute__((address_space(3)))
#define GAS __attribute__((address_space(1)))

constexpr int D = 640, H = 32, S = 8192, DH = 128, F = 1728, HD = H * DH;
constexpr int NWG = 256, NT = 512;
constexpr int NG = F / 64;
constexpr int NP = 32;
#define G0 4
#define GP 4
#define GPOLL0 2
constexpr int NGRP = (NP - G0 + GP - 1) / GP;
constexpr u32 SPIN_CAP = 1u << 18;
constexpr int AUX_NT = 2, AUX_SC1 = 16, RSRC_W3 = 0x00020000;

constexpr int WS_EPOCH = 0;
constexpr int WS_X1 = 4096;
constexpr int WS_X2 = WS_X1 + NWG * 256 * 8;
constexpr int WS_ML = WS_X2 + NWG * 128 * 8;
constexpr int WS_X3 = WS_ML + NWG * 128;
constexpr int WS_END = WS_X3 + 8 * 5 * 32 * 16 * 8;
constexpr int WS_STAMP = 2 << 20;

constexpr int L_WO = 0;
constexpr int L_W2 = 128 * 160;
constexpr int L_PART = L_W2 + 64 * 160;
constexpr int L_OP = L_PART + 8192;
constexpr int L_Q = L_OP + 4096;
constexpr int L_HID = L_Q + 256;
constexpr int L_MW = L_HID + 128;
constexpr int L_LW = L_MW + 32;
constexpr int L_FLAG = L_LW + 32;
constexpr int L_END = L_FLAG + 16;

struct Args {
  const unsigned short *x, *K, *V, *wq, *wo, *w1, *w3, *w2;
  unsigned short* out;
  unsigned char* ws;
};

__device__ __forceinline__ float asf(u32 u) { return __builtin_bit_cast(float, u); }
__device__ __forceinline__ u32 asu(float f) { return __builtin_bit_cast(u32, f); }
__device__ __forceinline__ float lo16(u32 u) { return asf(u << 16); }
__device__ __forceinline__ float hi16(u32 u) { return asf(u & 0xffff0000u); }
__device__ __forceinline__ u32 pack2(float a, float b) { f32x2 v = {a, b}; return __builtin_bit_cast(u32, __builtin_convertvector(v, bf16x2)); }
__device__ __forceinline__ float rbf(float a) { return (float)(__bf16)a; }
__device__ __forceinline__ float dot2(u32 a, u32 b, float c) {
  return __builtin_amdgcn_fdot2_f32_bf16(__builtin_bit_cast(bf16x2, a), __builtin_bit_cast(bf16x2, b), c, false);
}
template <int CTRL> __device__ __forceinline__ float dpp(float x) {
  return asf((u32)__builtin_amdgcn_mov_dpp((int)asu(x), CTRL, 0xf, 0xf, true));
}
constexpr int ROR1 = 0x121, ROR2 = 0x122, ROR4 = 0x124, ROR8 = 0x128;
__device__ __forceinline__ float row_sum(float x) { x += dpp<ROR8>(x); x += dpp<ROR4>(x); x += dpp<ROR2>(x); x += dpp<ROR1>(x); return x; }
__device__ __forceinline__ float swap16_sum(float x) { auto s = __builtin_amdgcn_permlane16_swap(asu(x), asu(x), false, false); return asf(s[0]) + asf(s[1]); }
__device__ __forceinline__ float swap32_sum(float x) { auto s = __builtin_amdgcn_permlane32_swap(asu(x), asu(x), false, false); return asf(s[0]) + asf(s[1]); }
__device__ __forceinline__ float xrow_sum(float x) { return swap32_sum(swap16_sum(x)); }
__device__ __forceinline__ float xrow_max(float x) {
  auto s = __builtin_amdgcn_permlane16_swap(asu(x), asu(x), false, false); x = fmaxf(asf(s[0]), asf(s[1]));
  auto t = __builtin_amdgcn_permlane32_swap(asu(x), asu(x), false, false); return fmaxf(asf(t[0]), asf(t[1]));
}
__device__ __forceinline__ float wave_sum(float x) { return xrow_sum(row_sum(x)); }
__device__ __forceinline__ f32x4 mfma(u32x4 a, u32x4 b, f32x4 c) {
  return __builtin_amdgcn_mfma_f32_16x16x32_bf16(__builtin_bit_cast(bf16x8, a), __builtin_bit_cast(bf16x8, b), c, 0, 0, 0);
}
__device__ __forceinline__ void lds_barrier() {
  asm volatile("s_waitcnt lgkmcnt(0)" ::: "memory");
  __builtin_amdgcn_s_barrier();
  asm volatile("" ::: "memory");
}
#define FENCE() do { asm volatile("" ::: "memory"); __builtin_amdgcn_sched_barrier(0); } while (0)

struct Tag { u32 lo, hi; };
__device__ __forceinline__ u32x4 gpack(float a, float b, Tag t) {
  u32x4 r; r.x = ((asu(a) + 0x80u) >> 8) | t.lo; r.y = t.hi; r.z = ((asu(b) + 0x80u) >> 8) | t.lo; r.w = t.hi; return r;
}
__device__ __forceinline__ u32 gbad(u32x4 g, Tag t) { return (((g.x ^ t.lo) | (g.z ^ t.lo)) >> 24) | (g.y ^ t.hi) | (g.w ^ t.hi); }
__device__ __forceinline__ float gval(u32 w) { return asf(w << 8); }

#define STAMP(i) do { } while (0)

__device__ __forceinline__ u32x4 bfrag(LAS unsigned char* img, int lane, int k0, int n0) {
  const int g = lane >> 4, q = (lane >> 2) & 3, p = lane & 3;
  LAS unsigned char* ad = img + (k0 + 8 * g + q) * 160 + (n0 + 4 * p) * 2;
  s16x4 t0 = __builtin_amdgcn_ds_read_tr16_b64_v4i16((LAS s16x4*)ad);
  s16x4 t1 = __builtin_amdgcn_ds_read_tr16_b64_v4i16((LAS s16x4*)(ad + 4 * 160));
  s16x8 r = __builtin_shufflevector(t0, t1, 0, 1, 2, 3, 4, 5, 6, 7);
  return __builtin_bit_cast(u32x4, r);
}

__global__ void __launch_bounds__(NT, 2) fused_kernel(Args a) {
  extern __shared__ __attribute__((aligned(16))) unsigned char smem[];
  LAS unsigned char* L = (LAS unsigned char*)smem;
  const int tid = threadIdx.x, lane = tid & 63, wave = __builtin_amdgcn_readfirstlane(tid >> 6);
  const int b = blockIdx.x, h = b >> 3, s = b & 7;
  const bool mlp = h < NG;
  asm volatile("" :: "s"(a.x), "s"(a.K), "s"(a.V), "s"(a.wq), "s"(a.wo), "s"(a.w1), "s"(a.w3), "s"(a.w2), "s"(a.out), "s"(a.ws));
  STAMP(0);

  const __amdgpu_buffer_rsrc_t rws = __builtin_amdgcn_make_buffer_rsrc((void*)a.ws, 0, WS_END, RSRC_W3);
  const __amdgpu_buffer_rsrc_t rk = __builtin_amdgcn_make_buffer_rsrc((void*)a.K, 0, H * S * DH * 2, RSRC_W3);
  const __amdgpu_buffer_rsrc_t rv = __builtin_amdgcn_make_buffer_rsrc((void*)a.V, 0, H * S * DH * 2, RSRC_W3);

  const u32x2 ep = __builtin_amdgcn_raw_buffer_load_b64(rws, WS_EPOCH, 0, AUX_SC1);
  u32 xs[5], xr[5];
#pragma unroll
  for (int i = 0; i < 5; ++i) xs[i] = ((const GAS u32*)a.x)[lane + 64 * i];
  const int prow = 80 * s + (tid >> 5), c32 = tid & 31;
#pragma unroll
  for (int i = 0; i < 5; ++i) xr[i] = ((const GAS unsigned short*)a.x)[prow + 16 * i];
  u32x4 wv[5];
  {
    const bool isq = c32 < 16 || !mlp;
    const GAS unsigned short* base = isq ? (const GAS unsigned short*)a.wq + h * DH + 8 * (c32 & 15)
                                         : (c32 < 24 ? (const GAS unsigned short*)a.w1 + 64 * h + 8 * (c32 - 16) : (const GAS unsigned short*)a.w3 + 64 * h + 8 * (c32 - 24));
    const int stride = isq ? HD : F;
    base += (size_t)prow * stride;
#pragma unroll
    for (int i = 0; i < 5; ++i) wv[i] = __builtin_nontemporal_load((const GAS u32x4*)(base + 16 * i * stride));
  }
  u32x4 wt[4];
#pragma unroll
  for (int j = 0; j < 4; ++j) {
    const int it = tid + 512 * j;
    const bool isw2 = it >= 1280 && it < 1920 && mlp;
    const int i2 = it < 1280 ? it : it - 1280;
    const int row = i2 / 10, ch = i2 - row * 10;
    const GAS unsigned short* p = isw2 ? (const GAS unsigned short*)a.w2 + (size_t)(64 * h + row) * D : (const GAS unsigned short*)a.wo + (size_t)(DH * h + row) * D;
    wt[j] = __builtin_nontemporal_load((const GAS u32x4*)(p + 80 * s + 8 * ch));
  }
  const u32 xo = ((const GAS u32*)a.x)[40 * s + 8 * (h < 5 ? h : 0) + (lane & 7)];
  FENCE();

  const int piece0 = h * 2048 + s * 256 + wave * NP;
  const int j0 = (h + 3 * s + 5 * wave) & (NP - 1);
  const int voff = lane * 16;
  u32x4 kv[NP];
#define POFF(i) ((piece0 + ((j0 + (i)) & (NP - 1))) << 10)
#pragma unroll
  for (int i = 0; i < G0; ++i) kv[i] = __builtin_amdgcn_raw_buffer_load_b128(rk, voff, POFF(i), AUX_NT);
  FENCE();

  float xn[5];
  {
    float sum = 0.f;
#pragma unroll
    for (int i = 0; i < 5; ++i) sum += lo16(xs[i]) + hi16(xs[i]);
    const float mean = wave_sum(sum) * (1.f / D);
    float sq = 0.f;
#pragma unroll
    for (int i = 0; i < 5; ++i) { const float d0 = lo16(xs[i]) - mean, d1 = hi16(xs[i]) - mean; sq += d0 * d0 + d1 * d1; }
    const float var = wave_sum(sq) * (1.f / D);
    const float mb = rbf(mean), sd = rbf(sqrtf(rbf(rbf(var) + 1e-5f)));
#pragma unroll
    for (int i = 0; i < 5; ++i) xn[i] = rbf(rbf(lo16(xr[i]) - mb) / sd);
  }
  {
    float acc[8];
#pragma unroll
    for (int j = 0; j < 8; ++j) acc[j] = 0.f;
#pragma unroll
    for (int i = 0; i < 5; ++i) {
      const u32 ph = asu(xn[i]), pl = ph >> 16;
#pragma unroll
      for (int j = 0; j < 4; ++j) { acc[2 * j] = dot2(wv[i][j], pl, acc[2 * j]); acc[2 * j + 1] = dot2(wv[i][j], ph, acc[2 * j + 1]); }
    }
#pragma unroll
    for (int j = 0; j < 8; ++j) acc[j] = swap32_sum(acc[j]);
    const bool up = lane >= 32;
    f32x4 o4 = {up ? acc[4] : acc[0], up ? acc[5] : acc[1], up ? acc[6] : acc[2], up ? acc[7] : acc[3]};
    *(LAS f32x4*)(L + L_PART + (wave * 256 + 8 * (lane & 31) + (up ? 4 : 0)) * 4) = o4;
    if (tid == 0) *(volatile LAS u32*)(L + L_FLAG) = 0u;
  }
  STAMP(1);
  lds_barrier();
  Tag tag;
  {
    const u32 e0 = __builtin_amdgcn_readfirstlane(ep.x), e1 = __builtin_amdgcn_readfirstlane(ep.y);
    const u64 e = (((u64)e1 << 32) | e0) + 1;
    tag.lo = (u32)(e & 0xff) << 24; tag.hi = (u32)(e >> 8);
  }
  if (wave == 0 || (wave == 1 && mlp)) {
    f32x2 sum = {0.f, 0.f};
#pragma unroll
    for (int w = 0; w < 8; ++w) sum += *(LAS f32x2*)(L + L_PART + (w * 256 + 2 * tid) * 4);
    __builtin_amdgcn_raw_buffer_store_b128(gpack(sum.x, sum.y, tag), rws, WS_X1 + (b * 256 + 2 * tid) * 8, 0, AUX_SC1);
  }
  STAMP(2);
  FENCE();

  bool mydone = !(wave == 0 || (wave == 1 && mlp));
  bool qready = false;
  const int x1off = WS_X1 + (h * 8 * 256 + wave * 128 + 2 * lane) * 8;
  auto finish_gather = [&](const u32x4 (&pq)[8]) {
    float s0 = 0.f, s1 = 0.f;
#pragma unroll
    for (int z = 0; z < 8; ++z) { s0 += gval(pq[z].x); s1 += gval(pq[z].z); }
    if (wave == 0) {
      *(volatile LAS u32*)(L + L_Q + lane * 4) = pack2(s0, s1);
      asm volatile("s_waitcnt lgkmcnt(0)" ::: "memory");
      *(volatile LAS u32*)(L + L_FLAG) = 1u;
    } else {
      const float g0 = rbf(s0), g1 = rbf(s1);
      auto t0 = __builtin_amdgcn_permlane32_swap(asu(g0), asu(g0), false, false);
      auto t1 = __builtin_amdgcn_permlane32_swap(asu(g1), asu(g1), false, false);
      const float ga = asf(t0[0]), ua = asf(t0[1]), gb = asf(t1[0]), ub = asf(t1[1]);
      const float ha = rbf(ga / (1.f + __expf(-ga))) * ua, hb = rbf(gb / (1.f + __expf(-gb))) * ub;
      if (lane < 32) *(volatile LAS u32*)(L + L_HID + lane * 4) = pack2(ha, hb);
    }
  };
#pragma unroll
  for (int g = 1; g <= NGRP; ++g) {
    const bool poll = !mydone && g >= GPOLL0;
    u32x4 pq[8];
    if (poll) {
#pragma unroll
      for (int z = 0; z < 8; ++z) pq[z] = __builtin_amdgcn_raw_buffer_load_b128(rws, x1off + z * 2048, 0, AUX_SC1);
    }
    FENCE();
#pragma unroll
    for (int i = G0 + (g - 1) * GP; i < G0 + g * GP && i < NP; ++i) kv[i] = __builtin_amdgcn_raw_buffer_load_b128(rk, voff, POFF(i), AUX_NT);
    FENCE();
    if (g == 1) {
#pragma unroll
      for (int j = 0; j < 4; ++j) if (tid + 512 * j < 1920) *(LAS u32x4*)(L + L_WO + (tid + 512 * j) * 16) = wt[j];
    }
    if (poll) {
      u32 bad = 0;
#pragma unroll
      for (int z = 0; z < 8; ++z) bad |= gbad(pq[z], tag);
      if (__builtin_amdgcn_ballot_w64(bad != 0) == 0) { finish_gather(pq); mydone = true; STAMP(10); }
    } else if (!qready) {
      asm volatile("s_waitcnt vmcnt(%0)" :: "n"(GP) : "memory");
    }
    if (!qready) qready = __builtin_amdgcn_readfirstlane(*(volatile LAS u32*)(L + L_FLAG)) != 0;
    FENCE();
  }
  if (!mydone) {
    for (u32 spin = 0; spin < SPIN_CAP; ++spin) {
      u32x4 pq[8];
      asm volatile("" ::: "memory");
#pragma unroll
      for (int z = 0; z < 8; ++z) pq[z] = __builtin_amdgcn_raw_buffer_load_b128(rws, x1off + z * 2048, 0, AUX_SC1);
      u32 bad = 0;
#pragma unroll
      for (int z = 0; z < 8; ++z) bad |= gbad(pq[z], tag);
      if (__builtin_amdgcn_ballot_w64(bad != 0) == 0) { finish_gather(pq); break; }
    }
  }
  if (!qready) {
    for (u32 spin = 0; spin < (SPIN_CAP << 4); ++spin) {
      if (__builtin_amdgcn_readfirstlane(*(volatile LAS u32*)(L + L_FLAG)) != 0) break;
      __builtin_amdgcn_s_sleep(1);
    }
  }
  asm volatile("" ::: "memory");
  const u32x4 qv = *(LAS u32x4*)(L + L_Q + (lane & 15) * 16);
  STAMP(3);
  FENCE();

  float sc[NP];
#pragma unroll
  for (int i = 0; i < NP; ++i) {
    float d = dot2(kv[i].x, qv.x, 0.f);
    d = dot2(kv[i].y, qv.y, d); d = dot2(kv[i].z, qv.z, d); d = dot2(kv[i].w, qv.w, d);
    kv[i] = __builtin_amdgcn_raw_buffer_load_b128(rv, voff, POFF(i), AUX_NT);
    d = row_sum(d);
    sc[i] = rbf(rbf(d) * 0.08838834764831845f);
    if ((i & 1) == 1) FENCE();
  }
  float mw = sc[0];
#pragma unroll
  for (int i = 1; i < NP; ++i) mw = fmaxf(mw, sc[i]);
  mw = xrow_max(mw);
  if (lane == 0) *(LAS float*)(L + L_MW + wave * 4) = mw;
  STAMP(4);
  lds_barrier();
  float M;
  {
    const f32x4 m0 = *(LAS f32x4*)(L + L_MW), m1 = *(LAS f32x4*)(L + L_MW + 16);
    M = fmaxf(fmaxf(fmaxf(m0.x, m0.y), fmaxf(m0.z, m0.w)), fmaxf(fmaxf(m1.x, m1.y), fmaxf(m1.z, m1.w)));
  }
  float lsum = 0.f;
#pragma unroll
  for (int i = 0; i < NP; ++i) {
    const float p = __builtin_amdgcn_exp2f((sc[i] - M) * 1.4426950408889634f);
    lsum += p;
    sc[i] = rbf(p);
  }
  lsum = xrow_sum(lsum);
  if (lane == 0) *(LAS float*)(L + L_LW + wave * 4) = lsum;
  FENCE();

  float acc[8];
#pragma unroll
  for (int j = 0; j < 8; ++j) acc[j] = 0.f;
#pragma unroll
  for (int i = 0; i < NP; ++i) {
    const u32 ph = asu(sc[i]), pl = ph >> 16;
#pragma unroll
    for (int j = 0; j < 4; ++j) { acc[2 * j] = dot2(kv[i][j], pl, acc[2 * j]); acc[2 * j + 1] = dot2(kv[i][j], ph, acc[2 * j + 1]); }
    if ((i & 1) == 1) FENCE();
  }
#pragma unroll
  for (int j = 0; j < 8; ++j) acc[j] = xrow_sum(acc[j]);
  if (lane < 32) {
    const bool up = lane >= 16;
    f32x4 o4 = {up ? acc[4] : acc[0], up ? acc[5] : acc[1], up ? acc[6] : acc[2], up ? acc[7] : acc[3]};
    *(LAS f32x4*)(L + L_OP + (wave * 128 + 8 * (lane & 15) + (up ? 4 : 0)) * 4) = o4;
  }
  STAMP(5);
  lds_barrier();
  if (wave != 0) return;

  {
    f32x2 o2 = {0.f, 0.f};
#pragma unroll
    for (int w = 0; w < 8; ++w) o2 += *(LAS f32x2*)(L + L_OP + (w * 128 + 2 * lane) * 4);
    __builtin_amdgcn_raw_buffer_store_b128(gpack(o2.x, o2.y, tag), rws, WS_X2 + (b * 128 + 2 * lane) * 8, 0, AUX_SC1);
    const f32x4 l0 = *(LAS f32x4*)(L + L_LW), l1 = *(LAS f32x4*)(L + L_LW + 16);
    const float lw = ((l0.x + l0.y) + (l0.z + l0.w)) + ((l1.x + l1.y) + (l1.z + l1.w));
    if (lane == 0) __builtin_amdgcn_raw_buffer_store_b128(gpack(M, lw, tag), rws, WS_ML + b * 128, 0, AUX_SC1);
  }
  STAMP(6);
  const int pz = lane & 7, half = (lane >> 3) & 1, g4 = lane >> 4;
  const int x2off = WS_X2 + ((h * 8 + pz) * 128 + 64 * half + 8 * g4) * 8;
  const int mloff = WS_ML + (h * 8 + pz) * 128;
  u32x4 ga[8], gm;
#define HOP1_LOADS() do { \
    _Pragma("unroll") for (int kk = 0; kk < 2; ++kk) \
      _Pragma("unroll") for (int u = 0; u < 4; ++u) ga[kk * 4 + u] = __builtin_amdgcn_raw_buffer_load_b128(rws, x2off + kk * 256 + u * 16, 0, AUX_SC1); \
    gm = __builtin_amdgcn_raw_buffer_load_b128(rws, mloff, 0, AUX_SC1); } while (0)
  HOP1_LOADS();
  FENCE();
  float yb[5];
  {
    if (mlp) {
      const u32x4 ha0 = *(LAS u32x4*)(L + L_HID + g4 * 16), ha1 = *(LAS u32x4*)(L + L_HID + 64 + g4 * 16);
#pragma unroll
      for (int t = 0; t < 5; ++t) {
        f32x4 d = {0.f, 0.f, 0.f, 0.f};
        d = mfma(ha0, bfrag(L + L_W2, lane, 0, 16 * t), d);
        d = mfma(ha1, bfrag(L + L_W2, lane, 32, 16 * t), d);
        yb[t] = d[0];
      }
    } else {
#pragma unroll
      for (int t = 0; t < 5; ++t) yb[t] = 0.f;
    }
  }
  u32x4 bf[5][4];
#pragma unroll
  for (int t = 0; t < 5; ++t)
#pragma unroll
    for (int ks = 0; ks < 4; ++ks) bf[t][ks] = bfrag(L + L_WO, lane, 32 * ks, 16 * t);
  FENCE();
  for (u32 spin = 0; spin < SPIN_CAP; ++spin) {
    u32 bad = gbad(gm, tag);
#pragma unroll
    for (int u = 0; u < 8; ++u) bad |= gbad(ga[u], tag);
    if (__builtin_amdgcn_ballot_w64(bad != 0) == 0) break;
    asm volatile("" ::: "memory");
    HOP1_LOADS();
  }
  STAMP(7);
  {
    const float mz = gval(gm.x), lz = gval(gm.z);
    float mx = mz;
    mx = fmaxf(mx, dpp<0xB1>(mx)); mx = fmaxf(mx, dpp<0x4E>(mx)); mx = fmaxf(mx, dpp<0x141>(mx));
    const float e = __builtin_amdgcn_exp2f((mz - mx) * 1.4426950408889634f);
    float ls = e * lz;
    ls += dpp<0xB1>(ls); ls += dpp<0x4E>(ls); ls += dpp<0x141>(ls);
    const float wz = e / ls;
    u32x4 af[2];
#pragma unroll
    for (int kk = 0; kk < 2; ++kk)
#pragma unroll
      for (int u = 0; u < 4; ++u) af[kk][u] = pack2(gval(ga[kk * 4 + u].x) * wz, gval(ga[kk * 4 + u].z) * wz);
    float y[5];
#pragma unroll
    for (int t = 0; t < 5; ++t) {
      f32x4 d1 = {0.f, 0.f, 0.f, 0.f}, d2 = {0.f, 0.f, 0.f, 0.f};
      d1 = mfma(af[0], bf[t][0], d1); d2 = mfma(af[0], bf[t][2], d2);
      d1 = mfma(af[1], bf[t][1], d1); d2 = mfma(af[1], bf[t][3], d2);
      const float u1 = (d1[0] + d1[1]) + (d1[2] + d1[3]), u2 = (d2[0] + d2[1]) + (d2[2] + d2[3]);
      y[t] = xrow_sum(lane < 32 ? u1 : u2) + yb[t];
    }
    const float ysel = g4 == 0 ? y[0] : g4 == 1 ? y[1] : g4 == 2 ? y[2] : y[3];
    const u32 thi = tag.hi;
    u32x2 gr; gr.x = ((asu(ysel) + 0x80u) >> 8) | tag.lo; gr.y = thi;
    __builtin_amdgcn_raw_buffer_store_b64(gr, rws, WS_X3 + (((s * 5 + g4) * 32 + h) * 16 + (lane & 15)) * 8, 0, AUX_SC1);
    if (lane < 16) {
      gr.x = ((asu(y[4]) + 0x80u) >> 8) | tag.lo;
      __builtin_amdgcn_raw_buffer_store_b64(gr, rws, WS_X3 + (((s * 5 + 4) * 32 + h) * 16 + lane) * 8, 0, AUX_SC1);
    }
  }
  STAMP(8);
  if (h >= 5) return;
  {
    const int x3off = WS_X3 + (((s * 5 + h) * 32 + (lane >> 3)) * 16 + 2 * (lane & 7)) * 8;
    u32x4 gy[4];
    for (u32 spin = 0; spin < SPIN_CAP; ++spin) {
      u32 bad = 0;
      asm volatile("" ::: "memory");
#pragma unroll
      for (int i = 0; i < 4; ++i) gy[i] = __builtin_amdgcn_raw_buffer_load_b128(rws, x3off + i * 1024, 0, AUX_SC1);
#pragma unroll
      for (int i = 0; i < 4; ++i) bad |= gbad(gy[i], tag);
      if (__builtin_amdgcn_ballot_w64(bad != 0) == 0) break;
    }
    float y0 = (gval(gy[0].x) + gval(gy[1].x)) + (gval(gy[2].x) + gval(gy[3].x));
    float y1 = (gval(gy[0].z) + gval(gy[1].z)) + (gval(gy[2].z) + gval(gy[3].z));
    y0 += dpp<ROR8>(y0); y1 += dpp<ROR8>(y1);
    y0 = xrow_sum(y0); y1 = xrow_sum(y1);
    if (lane < 8) ((GAS u32*)a.out)[40 * s + 8 * h + lane] = pack2(lo16(xo) + y0, hi16(xo) + y1);
    if (b == 0 && lane == 0) {
      const u64 full = ((((u64)ep.y << 32) | ep.x) + 1);
      __hip_atomic_store((GAS u64*)(a.ws + WS_EPOCH), full, __ATOMIC_RELAXED, __HIP_MEMORY_SCOPE_AGENT);
    }
  }
  STAMP(9);
}

}

extern "C" void kernel_launch(void* const* d_in, const int* in_sizes, int n_in, void* d_out, int out_size, void* d_ws, size_t ws_size, hipStream_t stream) {
  fused::Args a;
  a.x = (const unsigned short*)d_in[0];
  a.K = (const unsigned short*)d_in[1];
  a.V = (const unsigned short*)d_in[2];
  a.wq = (const unsigned short*)d_in[3];
  a.wo = (const unsigned short*)d_in[4];
  a.w1 = (const unsigned short*)d_in[6];
  a.w3 = (const unsigned short*)d_in[7];
  a.w2 = (const unsigned short*)d_in[8];
  a.out = (unsigned short*)d_out;
  a.ws = (unsigned char*)d_ws;
  hipLaunchKernelGGL(fused::fused_kernel, dim3(fused::NWG), dim3(fused::NT), fused::L_END, stream, a);
}
```

```cpp
#define PROBE_IN 1
#define PROBE_OFF 4288
#define PROBE_STRIDE 65408
#include <hip/hip_runtime.h>
#include <stdint.h>

namespace fused {

typedef unsigned u32;
typedef unsigned long long u64;
typedef u32 u32x2 __attribute__((ext_vector_type(2)));
typedef u32 u32x4 __attribute__((ext_vector_type(4)));
typedef float f32x2 __attribute__((ext_vector_type(2)));
typedef float f32x4 __attribute__((ext_vector_type(4)));
typedef short s16x4 __attribute__((ext_vector_type(4)));
typedef short s16x8 __attribute__((ext_vector_type(8)));
typedef __bf16 bf16x2 __attribute__((ext_vector_type(2)));
typedef __bf16 bf16x8 __attribute__((ext_vector_type(8)));
#define LAS __attribute__((address_space(3)))
#define GAS __attribute__((address_space(1)))

constexpr int D = 640, H = 32, S = 8192, DH = 128, F = 1728, HD = H * DH;
constexpr int NWG = 256, NT = 512;
constexpr int NG = F / 64;
constexpr int NP = 32;
#define G0 4
#define GP 4
#define GPOLL0 2
constexpr int NGRP = (NP - G0 + GP - 1) / GP;
constexpr u32 SPIN_CAP = 1u << 18;
constexpr int AUX_NT = 2, AUX_SC1 = 16, RSRC_W3 = 0x00020000;

constexpr int WS_EPOCH = 0;
constexpr int WS_X1 = 4096;
constexpr int WS_X2 = WS_X1 + NWG * 256 * 8;
constexpr int WS_ML = WS_X2 + NWG * 128 * 8;
constexpr int WS_X3 = WS_ML + NWG * 128;
constexpr int WS_END = WS_X3 + 8 * 5 * 32 * 16 * 8;
constexpr int WS_STAMP = 2 << 20;

constexpr int L_WO = 0;
constexpr int L_W2 = 128 * 160;
constexpr int L_PART = L_W2 + 64 * 160;
constexpr int L_OP = L_PART + 8192;
constexpr int L_Q = L_OP + 4096;
constexpr int L_HID = L_Q + 256;
constexpr int L_MW = L_HID + 128;
constexpr int L_LW = L_MW + 32;
constexpr int L_FLAG = L_LW + 32;
constexpr int L_END = L_FLAG + 16;

struct Args {
  const unsigned short *x, *K, *V, *wq, *wo, *w1, *w3, *w2;
  unsigned short* out;
  unsigned char* ws;
};

__device__ __forceinline__ float asf(u32 u) { return __builtin_bit_cast(float, u); }
__device__ __forceinline__ u32 asu(float f) { return __builtin_bit_cast(u32, f); }
__device__ __forceinline__ float lo16(u32 u) { return asf(u << 16); }
__device__ __forceinline__ float hi16(u32 u) { return asf(u & 0xffff0000u); }
__device__ __forceinline__ u32 pack2(float a, float b) { f32x2 v = {a, b}; return __builtin_bit_cast(u32, __builtin_convertvector(v, bf16x2)); }
__device__ __forceinline__ float rbf(float a) { return (float)(__bf16)a; }
__device__ __forceinline__ float dot2(u32 a, u32 b, float c) {
  return __builtin_amdgcn_fdot2_f32_bf16(__builtin_bit_cast(bf16x2, a), __builtin_bit_cast(bf16x2, b), c, false);
}
template <int CTRL> __device__ __forceinline__ float dpp(float x) {
  return asf((u32)__builtin_amdgcn_mov_dpp((int)asu(x), CTRL, 0xf, 0xf, true));
}
constexpr int ROR1 = 0x121, ROR2 = 0x122, ROR4 = 0x124, ROR8 = 0x128;
__device__ __forceinline__ float row_sum(float x) { x += dpp<ROR8>(x); x += dpp<ROR4>(x); x += dpp<ROR2>(x); x += dpp<ROR1>(x); return x; }
__device__ __forceinline__ float swap16_sum(float x) { auto s = __builtin_amdgcn_permlane16_swap(asu(x), asu(x), false, false); return asf(s[0]) + asf(s[1]); }
__device__ __forceinline__ float swap32_sum(float x) { auto s = __builtin_amdgcn_permlane32_swap(asu(x), asu(x), false, false); return asf(s[0]) + asf(s[1]); }
__device__ __forceinline__ float xrow_sum(float x) { return swap32_sum(swap16_sum(x)); }
__device__ __forceinline__ float xrow_max(float x) {
  auto s = __builtin_amdgcn_permlane16_swap(asu(x), asu(x), false, false); x = fmaxf(asf(s[0]), asf(s[1]));
  auto t = __builtin_amdgcn_permlane32_swap(asu(x), asu(x), false, false); return fmaxf(asf(t[0]), asf(t[1]));
}
__device__ __forceinline__ float wave_sum(float x) { return xrow_sum(row_sum(x)); }
__device__ __forceinline__ f32x4 mfma(u32x4 a, u32x4 b, f32x4 c) {
  return __builtin_amdgcn_mfma_f32_16x16x32_bf16(__builtin_bit_cast(bf16x8, a), __builtin_bit_cast(bf16x8, b), c, 0, 0, 0);
}
__device__ __forceinline__ void lds_barrier() {
  asm volatile("s_waitcnt lgkmcnt(0)" ::: "memory");
  __builtin_amdgcn_s_barrier();
  asm volatile("" ::: "memory");
}
#define FENCE() do { asm volatile("" ::: "memory"); __builtin_amdgcn_sched_barrier(0); } while (0)

struct Tag { u32 lo, hi; };
__device__ __forceinline__ u32x4 gpack(float a, float b, Tag t) {
  u32x4 r; r.x = ((asu(a) + 0x80u) >> 8) | t.lo; r.y = t.hi; r.z = ((asu(b) + 0x80u) >> 8) | t.lo; r.w = t.hi; return r;
}
__device__ __forceinline__ u32 gbad(u32x4 g, Tag t) { return (((g.x ^ t.lo) | (g.z ^ t.lo)) >> 24) | (g.y ^ t.hi) | (g.w ^ t.hi); }
__device__ __forceinline__ float gval(u32 w) { return asf(w << 8); }

#define STAMP(i) do { } while (0)

__device__ __forceinline__ u32x4 bfrag(LAS unsigned char* img, int lane, int k0, int n0) {
  const int g = lane >> 4, q = (lane >> 2) & 3, p = lane & 3;
  LAS unsigned char* ad = img + (k0 + 8 * g + q) * 160 + (n0 + 4 * p) * 2;
  s16x4 t0 = __builtin_amdgcn_ds_read_tr16_b64_v4i16((LAS s16x4*)ad);
  s16x4 t1 = __builtin_amdgcn_ds_read_tr16_b64_v4i16((LAS s16x4*)(ad + 4 * 160));
  s16x8 r = __builtin_shufflevector(t0, t1, 0, 1, 2, 3, 4, 5, 6, 7);
  return __builtin_bit_cast(u32x4, r);
}

__global__ void __launch_bounds__(NT, 2) fused_kernel(Args a) {
  extern __shared__ __attribute__((aligned(16))) unsigned char smem[];
  LAS unsigned char* L = (LAS unsigned char*)smem;
  const int tid = threadIdx.x, lane = tid & 63, wave = __builtin_amdgcn_readfirstlane(tid >> 6);
  const int b = blockIdx.x, h = b >> 3, s = b & 7;
  const bool mlp = h < NG;
  asm volatile("" :: "s"(a.x), "s"(a.K), "s"(a.V), "s"(a.wq), "s"(a.wo), "s"(a.w1), "s"(a.w3), "s"(a.w2), "s"(a.out), "s"(a.ws));
  STAMP(0);

  const __amdgpu_buffer_rsrc_t rws = __builtin_amdgcn_make_buffer_rsrc((void*)a.ws, 0, WS_END, RSRC_W3);
  const __amdgpu_buffer_rsrc_t rk = __builtin_amdgcn_make_buffer_rsrc((void*)a.K, 0, H * S * DH * 2, RSRC_W3);
  const __amdgpu_buffer_rsrc_t rv = __builtin_amdgcn_make_buffer_rsrc((void*)a.V, 0, H * S * DH * 2, RSRC_W3);

  const u32x2 ep = __builtin_amdgcn_raw_buffer_load_b64(rws, WS_EPOCH, 0, AUX_SC1);
  u32 xs[5], xr[5];
#pragma unroll
  for (int i = 0; i < 5; ++i) xs[i] = ((const GAS u32*)a.x)[lane + 64 * i];
  const int prow = 80 * s + (tid >> 5), c32 = tid & 31;
#pragma unroll
  for (int i = 0; i < 5; ++i) xr[i] = ((const GAS unsigned short*)a.x)[prow + 16 * i];
  u32x4 wv[5];
  {
    const bool isq = c32 < 16 || !mlp;
    const GAS unsigned short* base = isq ? (const GAS unsigned short*)a.wq + h * DH + 8 * (c32 & 15)
                                         : (c32 < 24 ? (const GAS unsigned short*)a.w1 + 64 * h + 8 * (c32 - 16) : (const GAS unsigned short*)a.w3 + 64 * h + 8 * (c32 - 24));
    const int stride = isq ? HD : F;
    base += (size_t)prow * stride;
#pragma unroll
    for (int i = 0; i < 5; ++i) wv[i] = __builtin_nontemporal_load((const GAS u32x4*)(base + 16 * i * stride));
  }
  u32x4 wt[4];
#pragma unroll
  for (int j = 0; j < 4; ++j) {
    const int it = tid + 512 * j;
    const bool isw2 = it >= 1280 && it < 1920 && mlp;
    const int i2 = it < 1280 ? it : it - 1280;
    const int row = i2 / 10, ch = i2 - row * 10;
    const GAS unsigned short* p = isw2 ? (const GAS unsigned short*)a.w2 + (size_t)(64 * h + row) * D : (const GAS unsigned short*)a.wo + (size_t)(DH * h + row) * D;
    wt[j] = __builtin_nontemporal_load((const GAS u32x4*)(p + 80 * s + 8 * ch));
  }
  const u32 xo = ((const GAS u32*)a.x)[40 * s + 8 * (h < 5 ? h : 0) + (lane & 7)];
  FENCE();

  const int piece0 = h * 2048 + s * 256 + wave * NP;
  const int j0 = (h + 3 * s + 5 * wave) & (NP - 1);
  const int voff = lane * 16;
  u32x4 kv[NP];
#define POFF(i) ((piece0 + ((j0 + (i)) & (NP - 1))) << 10)
#pragma unroll
  for (int i = 0; i < G0; ++i) kv[i] = __builtin_amdgcn_raw_buffer_load_b128(rk, voff, POFF(i), AUX_NT);
  FENCE();

  float xn[5];
  {
    float sum = 0.f;
#pragma unroll
    for (int i = 0; i < 5; ++i) sum += lo16(xs[i]) + hi16(xs[i]);
    const float mean = wave_sum(sum) * (1.f / D);
    float sq = 0.f;
#pragma unroll
    for (int i = 0; i < 5; ++i) { const float d0 = lo16(xs[i]) - mean, d1 = hi16(xs[i]) - mean; sq += d0 * d0 + d1 * d1; }
    const float var = wave_sum(sq) * (1.f / D);
    const float mb = rbf(mean), sd = rbf(sqrtf(rbf(rbf(var) + 1e-5f)));
#pragma unroll
    for (int i = 0; i < 5; ++i) xn[i] = rbf(rbf(lo16(xr[i]) - mb) / sd);
  }
  {
    float acc[8];
#pragma unroll
    for (int j = 0; j < 8; ++j) acc[j] = 0.f;
#pragma unroll
    for (int i = 0; i < 5; ++i) {
      const u32 ph = asu(xn[i]), pl = ph >> 16;
#pragma unroll
      for (int j = 0; j < 4; ++j) { acc[2 * j] = dot2(wv[i][j], pl, acc[2 * j]); acc[2 * j + 1] = dot2(wv[i][j], ph, acc[2 * j + 1]); }
    }
#pragma unroll
    for (int j = 0; j < 8; ++j) acc[j] = swap32_sum(acc[j]);
    const bool up = lane >= 32;
    f32x4 o4 = {up ? acc[4] : acc[0], up ? acc[5] : acc[1], up ? acc[6] : acc[2], up ? acc[7] : acc[3]};
    *(LAS f32x4*)(L + L_PART + (wave * 256 + 8 * (lane & 31) + (up ? 4 : 0)) * 4) = o4;
    if (tid == 0) *(volatile LAS u32*)(L + L_FLAG) = 0u;
  }
  STAMP(1);
  lds_barrier();
  Tag tag;
  {
    const u32 e0 = __builtin_amdgcn_readfirstlane(ep.x), e1 = __builtin_amdgcn_readfirstlane(ep.y);
    const u64 e = (((u64)e1 << 32) | e0) + 1;
    tag.lo = (u32)(e & 0xff) << 24; tag.hi = (u32)(e >> 8);
  }
  if (wave == 0 || (wave == 1 && mlp)) {
    f32x2 sum = {0.f, 0.f};
#pragma unroll
    for (int w = 0; w < 8; ++w) sum += *(LAS f32x2*)(L + L_PART + (w * 256 + 2 * tid) * 4);
    __builtin_amdgcn_raw_buffer_store_b128(gpack(sum.x, sum.y, tag), rws, WS_X1 + (b * 256 + 2 * tid) * 8, 0, AUX_SC1);
  }
  STAMP(2);
  FENCE();

  bool mydone = !(wave == 0 || (wave == 1 && mlp));
  bool qready = false;
  const int x1off = WS_X1 + (h * 8 * 256 + wave * 128 + 2 * lane) * 8;
  auto finish_gather = [&](const u32x4 (&pq)[8]) {
    float s0 = 0.f, s1 = 0.f;
#pragma unroll
    for (int z = 0; z < 8; ++z) { s0 += gval(pq[z].x); s1 += gval(pq[z].z); }
    if (wave == 0) {
      *(volatile LAS u32*)(L + L_Q + lane * 4) = pack2(s0, s1);
      asm volatile("s_waitcnt lgkmcnt(0)" ::: "memory");
      *(volatile LAS u32*)(L + L_FLAG) = 1u;
    } else {
      const float g0 = rbf(s0), g1 = rbf(s1);
      auto t0 = __builtin_amdgcn_permlane32_swap(asu(g0), asu(g0), false, false);
      auto t1 = __builtin_amdgcn_permlane32_swap(asu(g1), asu(g1), false, false);
      const float ga = asf(t0[0]), ua = asf(t0[1]), gb = asf(t1[0]), ub = asf(t1[1]);
      const float ha = rbf(ga / (1.f + __expf(-ga))) * ua, hb = rbf(gb / (1.f + __expf(-gb))) * ub;
      if (lane < 32) *(volatile LAS u32*)(L + L_HID + lane * 4) = pack2(ha, hb);
    }
  };
#pragma unroll
  for (int g = 1; g <= NGRP; ++g) {
    const bool poll = !mydone && g >= GPOLL0;
    u32x4 pq[8];
    if (poll) {
#pragma unroll
      for (int z = 0; z < 8; ++z) pq[z] = __builtin_amdgcn_raw_buffer_load_b128(rws, x1off + z * 2048, 0, AUX_SC1);
    }
    FENCE();
#pragma unroll
    for (int i = G0 + (g - 1) * GP; i < G0 + g * GP && i < NP; ++i) kv[i] = __builtin_amdgcn_raw_buffer_load_b128(rk, voff, POFF(i), AUX_NT);
    FENCE();
    if (g == 1) {
#pragma unroll
      for (int j = 0; j < 4; ++j) if (tid + 512 * j < 1920) *(LAS u32x4*)(L + L_WO + (tid + 512 * j) * 16) = wt[j];
    }
    if (poll) {
      u32 bad = 0;
#pragma unroll
      for (int z = 0; z < 8; ++z) bad |= gbad(pq[z], tag);
      if (__builtin_amdgcn_ballot_w64(bad != 0) == 0) { finish_gather(pq); mydone = true; STAMP(10); }
    } else if (!qready) {
      asm volatile("s_waitcnt vmcnt(%0)" :: "n"(GP) : "memory");
    }
    if (!qready) qready = __builtin_amdgcn_readfirstlane(*(volatile LAS u32*)(L + L_FLAG)) != 0;
    FENCE();
  }
  if (!mydone) {
    for (u32 spin = 0; spin < SPIN_CAP; ++spin) {
      u32x4 pq[8];
      asm volatile("" ::: "memory");
#pragma unroll
      for (int z = 0; z < 8; ++z) pq[z] = __builtin_amdgcn_raw_buffer_load_b128(rws, x1off + z * 2048, 0, AUX_SC1);
      u32 bad = 0;
#pragma unroll
      for (int z = 0; z < 8; ++z) bad |= gbad(pq[z], tag);
      if (__builtin_amdgcn_ballot_w64(bad != 0) == 0) { finish_gather(pq); break; }
    }
  }
  if (!qready) {
    for (u32 spin = 0; spin < (SPIN_CAP << 4); ++spin) {
      if (__builtin_amdgcn_readfirstlane(*(volatile LAS u32*)(L + L_FLAG)) != 0) break;
      __builtin_amdgcn_s_sleep(1);
    }
  }
  asm volatile("" ::: "memory");
  const u32x4 qv = *(LAS u32x4*)(L + L_Q + (lane & 15) * 16);
  STAMP(3);
  FENCE();

  float sc[NP];
#pragma unroll
  for (int i = 0; i < NP; ++i) {
    float d = dot2(kv[i].x, qv.x, 0.f);
    d = dot2(kv[i].y, qv.y, d); d = dot2(kv[i].z, qv.z, d); d = dot2(kv[i].w, qv.w, d);
    kv[i] = __builtin_amdgcn_raw_buffer_load_b128(rv, voff, POFF(i), AUX_NT);
    d = row_sum(d);
    sc[i] = rbf(rbf(d) * 0.08838834764831845f);
    if ((i & 1) == 1) FENCE();
  }
  float mw = sc[0];
#pragma unroll
  for (int i = 1; i < NP; ++i) mw = fmaxf(mw, sc[i]);
  mw = xrow_max(mw);
  if (lane == 0) *(LAS float*)(L + L_MW + wave * 4) = mw;
  STAMP(4);
  lds_barrier();
  float M;
  {
    const f32x4 m0 = *(LAS f32x4*)(L + L_MW), m1 = *(LAS f32x4*)(L + L_MW + 16);
    M = fmaxf(fmaxf(fmaxf(m0.x, m0.y), fmaxf(m0.z, m0.w)), fmaxf(fmaxf(m1.x, m1.y), fmaxf(m1.z, m1.w)));
  }
  float lsum = 0.f;
#pragma unroll
  for (int i = 0; i < NP; ++i) {
    const float p = __builtin_amdgcn_exp2f((sc[i] - M) * 1.4426950408889634f);
    lsum += p;
    sc[i] = rbf(p);
  }
  lsum = xrow_sum(lsum);
  if (lane == 0) *(LAS float*)(L + L_LW + wave * 4) = lsum;
  FENCE();

  float acc[8];
#pragma unroll
  for (int j = 0; j < 8; ++j) acc[j] = 0.f;
#pragma unroll
  for (int i = 0; i < NP; ++i) {
    const u32 ph = asu(sc[i]), pl = ph >> 16;
#pragma unroll
    for (int j = 0; j < 4; ++j) { acc[2 * j] = dot2(kv[i][j], pl, acc[2 * j]); acc[2 * j + 1] = dot2(kv[i][j], ph, acc[2 * j + 1]); }
    if ((i & 1) == 1) FENCE();
  }
#pragma unroll
  for (int j = 0; j < 8; ++j) acc[j] = xrow_sum(acc[j]);
  if (lane < 32) {
    const bool up = lane >= 16;
    f32x4 o4 = {up ? acc[4] : acc[0], up ? acc[5] : acc[1], up ? acc[6] : acc[2], up ? acc[7] : acc[3]};
    *(LAS f32x4*)(L + L_OP + (wave * 128 + 8 * (lane & 15) + (up ? 4 : 0)) * 4) = o4;
  }
  STAMP(5);
  lds_barrier();
  if (wave != 0) return;

  {
    f32x2 o2 = {0.f, 0.f};
#pragma unroll
    for (int w = 0; w < 8; ++w) o2 += *(LAS f32x2*)(L + L_OP + (w * 128 + 2 * lane) * 4);
    __builtin_amdgcn_raw_buffer_store_b128(gpack(o2.x, o2.y, tag), rws, WS_X2 + (b * 128 + 2 * lane) * 8, 0, AUX_SC1);
    const f32x4 l0 = *(LAS f32x4*)(L + L_LW), l1 = *(LAS f32x4*)(L + L_LW + 16);
    const float lw = ((l0.x + l0.y) + (l0.z + l0.w)) + ((l1.x + l1.y) + (l1.z + l1.w));
    if (lane == 0) __builtin_amdgcn_raw_buffer_store_b128(gpack(M, lw, tag), rws, WS_ML + b * 128, 0, AUX_SC1);
  }
  STAMP(6);
  const int pz = lane & 7, half = (lane >> 3) & 1, g4 = lane >> 4;
  const int x2off = WS_X2 + ((h * 8 + pz) * 128 + 64 * half + 8 * g4) * 8;
  const int mloff = WS_ML + (h * 8 + pz) * 128;
  u32x4 ga[8], gm;
#define HOP1_LOADS() do { \
    _Pragma("unroll") for (int kk = 0; kk < 2; ++kk) \
      _Pragma("unroll") for (int u = 0; u < 4; ++u) ga[kk * 4 + u] = __builtin_amdgcn_raw_buffer_load_b128(rws, x2off + kk * 256 + u * 16, 0, AUX_SC1); \
    gm = __builtin_amdgcn_raw_buffer_load_b128(rws, mloff, 0, AUX_SC1); } while (0)
  HOP1_LOADS();
  FENCE();
  float yb[5];
  {
    if (mlp) {
      const u32x4 ha0 = *(LAS u32x4*)(L + L_HID + g4 * 16), ha1 = *(LAS u32x4*)(L + L_HID + 64 + g4 * 16);
#pragma unroll
      for (int t = 0; t < 5; ++t) {
        f32x4 d = {0.f, 0.f, 0.f, 0.f};
        d = mfma(ha0, bfrag(L + L_W2, lane, 0, 16 * t), d);
        d = mfma(ha1, bfrag(L + L_W2, lane, 32, 16 * t), d);
        yb[t] = d[0];
      }
    } else {
#pragma unroll
      for (int t = 0; t < 5; ++t) yb[t] = 0.f;
    }
  }
  u32x4 bf[5][4];
#pragma unroll
  for (int t = 0; t < 5; ++t)
#pragma unroll
    for (int ks = 0; ks < 4; ++ks) bf[t][ks] = bfrag(L + L_WO, lane, 32 * ks, 16 * t);
  FENCE();
  for (u32 spin = 0; spin < SPIN_CAP; ++spin) {
    u32 bad = gbad(gm, tag);
#pragma unroll
    for (int u = 0; u < 8; ++u) bad |= gbad(ga[u], tag);
    if (__builtin_amdgcn_ballot_w64(bad != 0) == 0) break;
    asm volatile("" ::: "memory");
    HOP1_LOADS();
  }
  STAMP(7);
  {
    const float mz = gval(gm.x), lz = gval(gm.z);
    float mx = mz;
    mx = fmaxf(mx, dpp<0xB1>(mx)); mx = fmaxf(mx, dpp<0x4E>(mx)); mx = fmaxf(mx, dpp<0x141>(mx));
    const float e = __builtin_amdgcn_exp2f((mz - mx) * 1.4426950408889634f);
    float ls = e * lz;
    ls += dpp<0xB1>(ls); ls += dpp<0x4E>(ls); ls += dpp<0x141>(ls);
    const float wz = e / ls;
    u32x4 af[2];
#pragma unroll
    for (int kk = 0; kk < 2; ++kk)
#pragma unroll
      for (int u = 0; u < 4; ++u) af[kk][u] = pack2(gval(ga[kk * 4 + u].x) * wz, gval(ga[kk * 4 + u].z) * wz);
    float y[5];
#pragma unroll
    for (int t = 0; t < 5; ++t) {
      f32x4 d1 = {0.f, 0.f, 0.f, 0.f}, d2 = {0.f, 0.f, 0.f, 0.f};
      d1 = mfma(af[0], bf[t][0], d1); d2 = mfma(af[0], bf[t][2], d2);
      d1 = mfma(af[1], bf[t][1], d1); d2 = mfma(af[1], bf[t][3], d2);
      const float u1 = (d1[0] + d1[1]) + (d1[2] + d1[3]), u2 = (d2[0] + d2[1]) + (d2[2] + d2[3]);
      y[t] = xrow_sum(lane < 32 ? u1 : u2) + yb[t];
    }
    const float ysel = g4 == 0 ? y[0] : g4 == 1 ? y[1] : g4 == 2 ? y[2] : y[3];
    const u32 thi = tag.hi;
    u32x2 gr; gr.x = ((asu(ysel) + 0x80u) >> 8) | tag.lo; gr.y = thi;
    __builtin_amdgcn_raw_buffer_store_b64(gr, rws, WS_X3 + (((s * 5 + g4) * 32 + h) * 16 + (lane & 15)) * 8, 0, AUX_SC1);
    if (lane < 16) {
      gr.x = ((asu(y[4]) + 0x80u) >> 8) | tag.lo;
      __builtin_amdgcn_raw_buffer_store_b64(gr, rws, WS_X3 + (((s * 5 + 4) * 32 + h) * 16 + lane) * 8, 0, AUX_SC1);
    }
  }
  STAMP(8);
  if (h >= 5) return;
  {
    const int x3off = WS_X3 + (((s * 5 + h) * 32 + (lane >> 3)) * 16 + 2 * (lane & 7)) * 8;
    u32x4 gy[4];
    for (u32 spin = 0; spin < SPIN_CAP; ++spin) {
      u32 bad = 0;
      asm volatile("" ::: "memory");
#pragma unroll
      for (int i = 0; i < 4; ++i) gy[i] = __builtin_amdgcn_raw_buffer_load_b128(rws, x3off + i * 1024, 0, AUX_SC1);
#pragma unroll
      for (int i = 0; i < 4; ++i) bad |= gbad(gy[i], tag);
      if (__builtin_amdgcn_ballot_w64(bad != 0) == 0) break;
    }
    float y0 = (gval(gy[0].x) + gval(gy[1].x)) + (gval(gy[2].x) + gval(gy[3].x));
    float y1 = (gval(gy[0].z) + gval(gy[1].z)) + (gval(gy[2].z) + gval(gy[3].z));
    y0 += dpp<ROR8>(y0); y1 += dpp<ROR8>(y1);
    y0 = xrow_sum(y0); y1 = xrow_sum(y1);
    if (lane < 8) ((GAS u32*)a.out)[40 * s + 8 * h + lane] = pack2(lo16(xo) + y0, hi16(xo) + y1);
    if (b == 0 && lane == 0) {
      const u64 full = ((((u64)ep.y << 32) | ep.x) + 1);
      __hip_atomic_store((GAS u64*)(a.ws + WS_EPOCH), full, __ATOMIC_RELAXED, __HIP_MEMORY_SCOPE_AGENT);
    }
  }
  STAMP(9);
}

__global__ void probe_kernel(const unsigned char* base, unsigned stride, unsigned n, unsigned* sink) {
  unsigned off = 0, acc = 0;
  for (unsigned i = 0; i < n; ++i) {
    unsigned v = *(const volatile GAS unsigned*)(base + off);
    acc += v;
    unsigned z; asm volatile("v_and_b32 %0, 0, %1" : "=v"(z) : "v"(v));
    off += stride + z;
  }
  if (acc == 0x9e3779b9u) *sink = acc;
}
}

extern "C" void kernel_launch(void* const* d_in, const int* in_sizes, int n_in, void* d_out, int out_size, void* d_ws, size_t ws_size, hipStream_t stream) {
  fused::Args a;
  a.x = (const unsigned short*)d_in[0];
  a.K = (const unsigned short*)d_in[1];
  a.V = (const unsigned short*)d_in[2];
  a.wq = (const unsigned short*)d_in[3];
  a.wo = (const unsigned short*)d_in[4];
  a.w1 = (const unsigned short*)d_in[6];
  a.w3 = (const unsigned short*)d_in[7];
  a.w2 = (const unsigned short*)d_in[8];
  a.out = (unsigned short*)d_out;
  a.ws = (unsigned char*)d_ws;
  hipLaunchKernelGGL(fused::probe_kernel, dim3(1), dim3(1), 0, stream, (const unsigned char*)d_in[PROBE_IN] + (size_t)PROBE_OFF, (unsigned)PROBE_STRIDE, 256u, (unsigned*)((char*)d_ws + (8 << 20)));
  hipLaunchKernelGGL(fused::fused_kernel, dim3(fused::NWG), dim3(fused::NT), fused::L_END, stream, a);
}
```

```cpp
#include <hip/hip_runtime.h>
#include <stdint.h>

namespace fused {

typedef unsigned u32;
typedef unsigned long long u64;
typedef u32 u32x2 __attribute__((ext_vector_type(2)));
typedef u32 u32x4 __attribute__((ext_vector_type(4)));
typedef float f32x2 __attribute__((ext_vector_type(2)));
typedef float f32x4 __attribute__((ext_vector_type(4)));
typedef short s16x4 __attribute__((ext_vector_type(4)));
typedef short s16x8 __attribute__((ext_vector_type(8)));
typedef __bf16 bf16x2 __attribute__((ext_vector_type(2)));
typedef __bf16 bf16x8 __attribute__((ext_vector_type(8)));
#define LAS __attribute__((address_space(3)))
#define GAS __attribute__((address_space(1)))

constexpr int D = 640, H = 32, S = 8192, DH = 128, F = 1728, HD = H * DH;
constexpr int NWG = 256, NT = 512;
constexpr int NG = F / 64;
constexpr int NP = 32;
#define G0 4
#define GP 4
#define GPOLL0 2
constexpr int NGRP = (NP - G0 + GP - 1) / GP;
constexpr u32 SPIN_CAP = 1u << 18;
constexpr int AUX_NT = 2, AUX_SC1 = 16, RSRC_W3 = 0x00020000;

constexpr int WS_EPOCH = 0;
constexpr int WS_X1 = 4096;
constexpr int WS_X2 = WS_X1 + NWG * 256 * 8;
constexpr int WS_ML = WS_X2 + NWG * 128 * 8;
constexpr int WS_X3 = WS_ML + NWG * 128;
constexpr int WS_END = WS_X3 + 8 * 5 * 32 * 16 * 8;
constexpr int WS_STAMP = 2 << 20;

constexpr int L_WO = 0;
constexpr int L_W2 = 128 * 160;
constexpr int L_PART = L_W2 + 64 * 160;
constexpr int L_OP = L_PART + 8192;
constexpr int L_Q = L_OP + 4096;
constexpr int L_HID = L_Q + 256;
constexpr int L_MW = L_HID + 128;
constexpr int L_LW = L_MW + 32;
constexpr int L_FLAG = L_LW + 32;
constexpr int L_END = L_FLAG + 16;

struct Args {
  const unsigned short *x, *K, *V, *wq, *wo, *w1, *w3, *w2;
  unsigned short* out;
  unsigned char* ws;
};

__device__ __forceinline__ float asf(u32 u) { return __builtin_bit_cast(float, u); }
__device__ __forceinline__ u32 asu(float f) { return __builtin_bit_cast(u32, f); }
__device__ __forceinline__ float lo16(u32 u) { return asf(u << 16); }
__device__ __forceinline__ float hi16(u32 u) { return asf(u & 0xffff0000u); }
__device__ __forceinline__ u32 pack2(float a, float b) { f32x2 v = {a, b}; return __builtin_bit_cast(u32, __builtin_convertvector(v, bf16x2)); }
__device__ __forceinline__ float rbf(float a) { return (float)(__bf16)a; }
__device__ __forceinline__ float dot2(u32 a, u32 b, float c) {
  return __builtin_amdgcn_fdot2_f32_bf16(__builtin_bit_cast(bf16x2, a), __builtin_bit_cast(bf16x2, b), c, false);
}
template <int CTRL> __device__ __forceinline__ float dpp(float x) {
  return asf((u32)__builtin_amdgcn_mov_dpp((int)asu(x), CTRL, 0xf, 0xf, true));
}
constexpr int ROR1 = 0x121, ROR2 = 0x122, ROR4 = 0x124, ROR8 = 0x128;
__device__ __forceinline__ float row_sum(float x) { x += dpp<ROR8>(x); x += dpp<ROR4>(x); x += dpp<ROR2>(x); x += dpp<ROR1>(x); return x; }
__device__ __forceinline__ float swap16_sum(float x) { auto s = __builtin_amdgcn_permlane16_swap(asu(x), asu(x), false, false); return asf(s[0]) + asf(s[1]); }
__device__ __forceinline__ float swap32_sum(float x) { auto s = __builtin_amdgcn_permlane32_swap(asu(x), asu(x), false, false); return asf(s[0]) + asf(s[1]); }
__device__ __forceinline__ float xrow_sum(float x) { return swap32_sum(swap16_sum(x)); }
__device__ __forceinline__ float xrow_max(float x) {
  auto s = __builtin_amdgcn_permlane16_swap(asu(x), asu(x), false, false); x = fmaxf(asf(s[0]), asf(s[1]));
  auto t = __builtin_amdgcn_permlane32_swap(asu(x), asu(x), false, false); return fmaxf(asf(t[0]), asf(t[1]));
}
__device__ __forceinline__ float wave_sum(float x) { return xrow_sum(row_sum(x)); }
__device__ __forceinline__ f32x4 mfma(u32x4 a, u32x4 b, f32x4 c) {
  return __builtin_amdgcn_mfma_f32_16x16x32_bf16(__builtin_bit_cast(bf16x8, a), __builtin_bit_cast(bf16x8, b), c, 0, 0, 0);
}
__device__ __forceinline__ void lds_barrier() {
  asm volatile("s_waitcnt lgkmcnt(0)" ::: "memory");
  __builtin_amdgcn_s_barrier();
  asm volatile("" ::: "memory");
}
#define FENCE() do { asm volatile("" ::: "memory"); __builtin_amdgcn_sched_barrier(0); } while (0)

struct Tag { u32 lo, hi; };
__device__ __forceinline__ u32x4 gpack(float a, float b, Tag t) {
  u32x4 r; r.x = ((asu(a) + 0x80u) >> 8) | t.lo; r.y = t.hi; r.z = ((asu(b) + 0x80u) >> 8) | t.lo; r.w = t.hi; return r;
}
__device__ __forceinline__ u32 gbad(u32x4 g, Tag t) { return (((g.x ^ t.lo) | (g.z ^ t.lo)) >> 24) | (g.y ^ t.hi) | (g.w ^ t.hi); }
__device__ __forceinline__ float gval(u32 w) { return asf(w << 8); }

#define STAMP(i) do { } while (0)

__device__ __forceinline__ u32x4 bfrag(LAS unsigned char* img, int lane, int k0, int n0) {
  const int g = lane >> 4, q = (lane >> 2) & 3, p = lane & 3;
  LAS unsigned char* ad = img + (k0 + 8 * g + q) * 160 + (n0 + 4 * p) * 2;
  s16x4 t0 = __builtin_amdgcn_ds_read_tr16_b64_v4i16((LAS s16x4*)ad);
  s16x4 t1 = __builtin_amdgcn_ds_read_tr16_b64_v4i16((LAS s16x4*)(ad + 4 * 160));
  s16x8 r = __builtin_shufflevector(t0, t1, 0, 1, 2, 3, 4, 5, 6, 7);
  return __builtin_bit_cast(u32x4, r);
}

__global__ void __launch_bounds__(NT, 2) fused_kernel(Args a) {
  extern __shared__ __attribute__((aligned(16))) unsigned char smem[];
  LAS unsigned char* L = (LAS unsigned char*)smem;
  const int tid = threadIdx.x, lane = tid & 63, wave = __builtin_amdgcn_readfirstlane(tid >> 6);
  const int b = blockIdx.x, h = b >> 3, s = b & 7;
  const bool mlp = h < NG;
  asm volatile("" :: "s"(a.x), "s"(a.K), "s"(a.V), "s"(a.wq), "s"(a.wo), "s"(a.w1), "s"(a.w3), "s"(a.w2), "s"(a.out), "s"(a.ws));
  STAMP(0);

  const __amdgpu_buffer_rsrc_t rws = __builtin_amdgcn_make_buffer_rsrc((void*)a.ws, 0, WS_END, RSRC_W3);
  const __amdgpu_buffer_rsrc_t rk = __builtin_amdgcn_make_buffer_rsrc((void*)a.K, 0, H * S * DH * 2, RSRC_W3);
  const __amdgpu_buffer_rsrc_t rv = __builtin_amdgcn_make_buffer_rsrc((void*)a.V, 0, H * S * DH * 2, RSRC_W3);

  const u32x2 ep = __builtin_amdgcn_raw_buffer_load_b64(rws, WS_EPOCH, 0, AUX_SC1);
  u32 xs[5], xr[5];
#pragma unroll
  for (int i = 0; i < 5; ++i) xs[i] = ((const GAS u32*)a.x)[lane + 64 * i];
  const int prow = 80 * s + (tid >> 5), c32 = tid & 31;
#pragma unroll
  for (int i = 0; i < 5; ++i) xr[i] = ((const GAS unsigned short*)a.x)[prow + 16 * i];
  u32x4 wv[5];
  {
    const bool isq = c32 < 16 || !mlp;
    const GAS unsigned short* base = isq ? (const GAS unsigned short*)a.wq + h * DH + 8 * (c32 & 15)
                                         : (c32 < 24 ? (const GAS unsigned short*)a.w1 + 64 * h + 8 * (c32 - 16) : (const GAS unsigned short*)a.w3 + 64 * h + 8 * (c32 - 24));
    const int stride = isq ? HD : F;
    base += (size_t)prow * stride;
#pragma unroll
    for (int i = 0; i < 5; ++i) wv[i] = __builtin_nontemporal_load((const GAS u32x4*)(base + 16 * i * stride));
  }
  u32x4 wt[4];
#pragma unroll
  for (int j = 0; j < 4; ++j) {
    const int it = tid + 512 * j;
    const bool isw2 = it >= 1280 && it < 1920 && mlp;
    const int i2 = it < 1280 ? it : it - 1280;
    const int row = i2 / 10, ch = i2 - row * 10;
    const GAS unsigned short* p = isw2 ? (const GAS unsigned short*)a.w2 + (size_t)(64 * h + row) * D : (const GAS unsigned short*)a.wo + (size_t)(DH * h + row) * D;
    wt[j] = __builtin_nontemporal_load((const GAS u32x4*)(p + 80 * s + 8 * ch));
  }
  const u32 xo = ((const GAS u32*)a.x)[40 * s + 8 * (h < 5 ? h : 0) + (lane & 7)];
  FENCE();

  const int piece0 = h * 2048 + s * 256 + wave * NP;
  const int j0 = (h + 3 * s + 5 * wave) & (NP - 1);
  const int voff = lane * 16;
  u32x4 kv[NP];
#define POFF(i) ((piece0 + ((j0 + (i)) & (NP - 1))) << 10)
#pragma unroll
  for (int i = 0; i < G0; ++i) kv[i] = __builtin_amdgcn_raw_buffer_load_b128(rk, voff, POFF(i), AUX_NT);
  FENCE();

  float xn[5];
  {
    float sum = 0.f;
#pragma unroll
    for (int i = 0; i < 5; ++i) sum += lo16(xs[i]) + hi16(xs[i]);
    const float mean = wave_sum(sum) * (1.f / D);
    float sq = 0.f;
#pragma unroll
    for (int i = 0; i < 5; ++i) { const float d0 = lo16(xs[i]) - mean, d1 = hi16(xs[i]) - mean; sq += d0 * d0 + d1 * d1; }
    const float var = wave_sum(sq) * (1.f / D);
    const float mb = rbf(mean), sd = rbf(sqrtf(rbf(rbf(var) + 1e-5f)));
#pragma unroll
    for (int i = 0; i < 5; ++i) xn[i] = rbf(rbf(lo16(xr[i]) - mb) / sd);
  }
  {
    float acc[8];
#pragma unroll
    for (int j = 0; j < 8; ++j) acc[j] = 0.f;
#pragma unroll
    for (int i = 0; i < 5; ++i) {
      const u32 ph = asu(xn[i]), pl = ph >> 16;
#pragma unroll
      for (int j = 0; j < 4; ++j) { acc[2 * j] = dot2(wv[i][j], pl, acc[2 * j]); acc[2 * j + 1] = dot2(wv[i][j], ph, acc[2 * j + 1]); }
    }
#pragma unroll
    for (int j = 0; j < 8; ++j) acc[j] = swap32_sum(acc[j]);
    const bool up = lane >= 32;
    f32x4 o4 = {up ? acc[4] : acc[0], up ? acc[5] : acc[1], up ? acc[6] : acc[2], up ? acc[7] : acc[3]};
    *(LAS f32x4*)(L + L_PART + (wave * 256 + 8 * (lane & 31) + (up ? 4 : 0)) * 4) = o4;
    if (tid == 0) *(volatile LAS u32*)(L + L_FLAG) = 0u;
  }
  STAMP(1);
  lds_barrier();
  Tag tag;
  {
    const u32 e0 = __builtin_amdgcn_readfirstlane(ep.x), e1 = __builtin_amdgcn_readfirstlane(ep.y);
    const u64 e = (((u64)e1 << 32) | e0) + 1;
    tag.lo = (u32)(e & 0xff) << 24; tag.hi = (u32)(e >> 8);
  }
  if (wave == 0 || (wave == 1 && mlp)) {
    f32x2 sum = {0.f, 0.f};
#pragma unroll
    for (int w = 0; w < 8; ++w) sum += *(LAS f32x2*)(L + L_PART + (w * 256 + 2 * tid) * 4);
    __builtin_amdgcn_raw_buffer_store_b128(gpack(sum.x, sum.y, tag), rws, WS_X1 + (b * 256 + 2 * tid) * 8, 0, AUX_SC1);
  }
  STAMP(2);
  FENCE();

  bool mydone = !(wave == 0 || (wave == 1 && mlp));
  bool qready = false;
  const int x1off = WS_X1 + (h * 8 * 256 + wave * 128 + 2 * lane) * 8;
  auto finish_gather = [&](const u32x4 (&pq)[8]) {
    float s0 = 0.f, s1 = 0.f;
#pragma unroll
    for (int z = 0; z < 8; ++z) { s0 += gval(pq[z].x); s1 += gval(pq[z].z); }
    if (wave == 0) {
      *(volatile LAS u32*)(L + L_Q + lane * 4) = pack2(s0, s1);
      asm volatile("s_waitcnt lgkmcnt(0)" ::: "memory");
      *(volatile LAS u32*)(L + L_FLAG) = 1u;
    } else {
      const float g0 = rbf(s0), g1 = rbf(s1);
      auto t0 = __builtin_amdgcn_permlane32_swap(asu(g0), asu(g0), false, false);
      auto t1 = __builtin_amdgcn_permlane32_swap(asu(g1), asu(g1), false, false);
      const float ga = asf(t0[0]), ua = asf(t0[1]), gb = asf(t1[0]), ub = asf(t1[1]);
      const float ha = rbf(ga / (1.f + __expf(-ga))) * ua, hb = rbf(gb / (1.f + __expf(-gb))) * ub;
      if (lane < 32) *(volatile LAS u32*)(L + L_HID + lane * 4) = pack2(ha, hb);
    }
  };
#pragma unroll
  for (int g = 1; g <= NGRP; ++g) {
    const bool poll = !mydone && g >= GPOLL0;
    u32x4 pq[8];
    if (poll) {
#pragma unroll
      for (int z = 0; z < 8; ++z) pq[z] = __builtin_amdgcn_raw_buffer_load_b128(rws, x1off + z * 2048, 0, AUX_SC1);
    }
    FENCE();
#pragma unroll
    for (int i = G0 + (g - 1) * GP; i < G0 + g * GP && i < NP; ++i) kv[i] = __builtin_amdgcn_raw_buffer_load_b128(rk, voff, POFF(i), AUX_NT);
    FENCE();
    if (g == 1) {
#pragma unroll
      for (int j = 0; j < 4; ++j) if (tid + 512 * j < 1920) *(LAS u32x4*)(L + L_WO + (tid + 512 * j) * 16) = wt[j];
    }
    if (poll) {
      u32 bad = 0;
#pragma unroll
      for (int z = 0; z < 8; ++z) bad |= gbad(pq[z], tag);
      if (__builtin_amdgcn_ballot_w64(bad != 0) == 0) { finish_gather(pq); mydone = true; STAMP(10); }
    } else if (!qready) {
      asm volatile("s_waitcnt vmcnt(%0)" :: "n"(GP) : "memory");
    }
    if (!qready) qready = __builtin_amdgcn_readfirstlane(*(volatile LAS u32*)(L + L_FLAG)) != 0;
    FENCE();
  }
  if (!mydone) {
    for (u32 spin = 0; spin < SPIN_CAP; ++spin) {
      u32x4 pq[8];
      asm volatile("" ::: "memory");
#pragma unroll
      for (int z = 0; z < 8; ++z) pq[z] = __builtin_amdgcn_raw_buffer_load_b128(rws, x1off + z * 2048, 0, AUX_SC1);
      u32 bad = 0;
#pragma unroll
      for (int z = 0; z < 8; ++z) bad |= gbad(pq[z], tag);
      if (__builtin_amdgcn_ballot_w64(bad != 0) == 0) { finish_gather(pq); break; }
    }
  }
  if (!qready) {
    for (u32 spin = 0; spin < (SPIN_CAP << 4); ++spin) {
      if (__builtin_amdgcn_readfirstlane(*(volatile LAS u32*)(L + L_FLAG)) != 0) break;
      __builtin_amdgcn_s_sleep(1);
    }
  }
  asm volatile("" ::: "memory");
  const u32x4 qv = *(LAS u32x4*)(L + L_Q + (lane & 15) * 16);
  STAMP(3);
  FENCE();

  float sc[NP];
#pragma unroll
  for (int i = 0; i < NP; ++i) {
    float d = dot2(kv[i].x, qv.x, 0.f);
    d = dot2(kv[i].y, qv.y, d); d = dot2(kv[i].z, qv.z, d); d = dot2(kv[i].w, qv.w, d);
    kv[i] = __builtin_amdgcn_raw_buffer_load_b128(rv, voff, POFF(i), AUX_NT);
    d = row_sum(d);
    sc[i] = rbf(rbf(d) * 0.08838834764831845f);
    if ((i & 1) == 1) FENCE();
  }
  float mw = sc[0];
#pragma unroll
  for (int i = 1; i < NP; ++i) mw = fmaxf(mw, sc[i]);
  mw = xrow_max(mw);
  if (lane == 0) *(LAS float*)(L + L_MW + wave * 4) = mw;
  STAMP(4);
  lds_barrier();
  float M;
  {
    const f32x4 m0 = *(LAS f32x4*)(L + L_MW), m1 = *(LAS f32x4*)(L + L_MW + 16);
    M = fmaxf(fmaxf(fmaxf(m0.x, m0.y), fmaxf(m0.z, m0.w)), fmaxf(fmaxf(m1.x, m1.y), fmaxf(m1.z, m1.w)));
  }
  float lsum = 0.f;
#pragma unroll
  for (int i = 0; i < NP; ++i) {
    const float p = __builtin_amdgcn_exp2f((sc[i] - M) * 1.4426950408889634f);
    lsum += p;
    sc[i] = rbf(p);
  }
  lsum = xrow_sum(lsum);
  if (lane == 0) *(LAS float*)(L + L_LW + wave * 4) = lsum;
  FENCE();

  float acc[8];
#pragma unroll
  for (int j = 0; j < 8; ++j) acc[j] = 0.f;
#pragma unroll
  for (int i = 0; i < NP; ++i) {
    const u32 ph = asu(sc[i]), pl = ph >> 16;
#pragma unroll
    for (int j = 0; j < 4; ++j) { acc[2 * j] = dot2(kv[i][j], pl, acc[2 * j]); acc[2 * j + 1] = dot2(kv[i][j], ph, acc[2 * j + 1]); }
    if ((i & 1) == 1) FENCE();
  }
#pragma unroll
  for (int j = 0; j < 8; ++j) acc[j] = xrow_sum(acc[j]);
  if (lane < 32) {
    const bool up = lane >= 16;
    f32x4 o4 = {up ? acc[4] : acc[0], up ? acc[5] : acc[1], up ? acc[6] : acc[2], up ? acc[7] : acc[3]};
    *(LAS f32x4*)(L + L_OP + (wave * 128 + 8 * (lane & 15) + (up ? 4 : 0)) * 4) = o4;
  }
  STAMP(5);
  lds_barrier();
  if (wave != 0) return;

  {
    f32x2 o2 = {0.f, 0.f};
#pragma unroll
    for (int w = 0; w < 8; ++w) o2 += *(LAS f32x2*)(L + L_OP + (w * 128 + 2 * lane) * 4);
    __builtin_amdgcn_raw_buffer_store_b128(gpack(o2.x, o2.y, tag), rws, WS_X2 + (b * 128 + 2 * lane) * 8, 0, AUX_SC1);
    const f32x4 l0 = *(LAS f32x4*)(L + L_LW), l1 = *(LAS f32x4*)(L + L_LW + 16);
    const float lw = ((l0.x + l0.y) + (l0.z + l0.w)) + ((l1.x + l1.y) + (l1.z + l1.w));
    if (lane == 0) __builtin_amdgcn_raw_buffer_store_b128(gpack(M, lw, tag), rws, WS_ML + b * 128, 0, AUX_SC1);
  }
  STAMP(6);
  const int pz = lane & 7, half = (lane >> 3) & 1, g4 = lane >> 4;
  const int x2off = WS_X2 + ((h * 8 + pz) * 128 + 64 * half + 8 * g4) * 8;
  const int mloff = WS_ML + (h * 8 + pz) * 128;
  u32x4 ga[8], gm;
#define HOP1_LOADS() do { \
    _Pragma("unroll") for (int kk = 0; kk < 2; ++kk) \
      _Pragma("unroll") for (int u = 0; u < 4; ++u) ga[kk * 4 + u] = __builtin_amdgcn_raw_buffer_load_b128(rws, x2off + kk * 256 + u * 16, 0, AUX_SC1); \
    gm = __builtin_amdgcn_raw_buffer_load_b128(rws, mloff, 0, AUX_SC1); } while (0)
  HOP1_LOADS();
  FENCE();
  float yb[5];
  {
    if (mlp) {
      const u32x4 ha0 = *(LAS u32x4*)(L + L_HID + g4 * 16), ha1 = *(LAS u32x4*)(L + L_HID + 64 + g4 * 16);
#pragma unroll
      for (int t = 0; t < 5; ++t) {
        f32x4 d = {0.f, 0.f, 0.f, 0.f};
        d = mfma(ha0, bfrag(L + L_W2, lane, 0, 16 * t), d);
        d = mfma(ha1, bfrag(L + L_W2, lane, 32, 16 * t), d);
        yb[t] = d[0];
      }
    } else {
#pragma unroll
      for (int t = 0; t < 5; ++t) yb[t] = 0.f;
    }
  }
  u32x4 bf[5][4];
#pragma unroll
  for (int t = 0; t < 5; ++t)
#pragma unroll
    for (int ks = 0; ks < 4; ++ks) bf[t][ks] = bfrag(L + L_WO, lane, 32 * ks, 16 * t);
  FENCE();
  for (u32 spin = 0; spin < SPIN_CAP; ++spin) {
    u32 bad = gbad(gm, tag);
#pragma unroll
    for (int u = 0; u < 8; ++u) bad |= gbad(ga[u], tag);
    if (__builtin_amdgcn_ballot_w64(bad != 0) == 0) break;
    asm volatile("" ::: "memory");
    HOP1_LOADS();
  }
  STAMP(7);
  {
    const float mz = gval(gm.x), lz = gval(gm.z);
    float mx = mz;
    mx = fmaxf(mx, dpp<0xB1>(mx)); mx = fmaxf(mx, dpp<0x4E>(mx)); mx = fmaxf(mx, dpp<0x141>(mx));
    const float e = __builtin_amdgcn_exp2f((mz - mx) * 1.4426950408889634f);
    float ls = e * lz;
    ls += dpp<0xB1>(ls); ls += dpp<0x4E>(ls); ls += dpp<0x141>(ls);
    const float wz = e / ls;
    u32x4 af[2];
#pragma unroll
    for (int kk = 0; kk < 2; ++kk)
#pragma unroll
      for (int u = 0; u < 4; ++u) af[kk][u] = pack2(gval(ga[kk * 4 + u].x) * wz, gval(ga[kk * 4 + u].z) * wz);
    float y[5];
#pragma unroll
    for (int t = 0; t < 5; ++t) {
      f32x4 d1 = {0.f, 0.f, 0.f, 0.f}, d2 = {0.f, 0.f, 0.f, 0.f};
      d1 = mfma(af[0], bf[t][0], d1); d2 = mfma(af[0], bf[t][2], d2);
      d1 = mfma(af[1], bf[t][1], d1); d2 = mfma(af[1], bf[t][3], d2);
      const float u1 = (d1[0] + d1[1]) + (d1[2] + d1[3]), u2 = (d2[0] + d2[1]) + (d2[2] + d2[3]);
      y[t] = xrow_sum(lane < 32 ? u1 : u2) + yb[t];
    }
    const float ysel = g4 == 0 ? y[0] : g4 == 1 ? y[1] : g4 == 2 ? y[2] : y[3];
    const u32 thi = tag.hi;
    u32x2 gr; gr.x = ((asu(ysel) + 0x80u) >> 8) | tag.lo; gr.y = thi;
    __builtin_amdgcn_raw_buffer_store_b64(gr, rws, WS_X3 + (((s * 5 + g4) * 32 + h) * 16 + (lane & 15)) * 8, 0, AUX_SC1);
    if (lane < 16) {
      gr.x = ((asu(y[4]) + 0x80u) >> 8) | tag.lo;
      __builtin_amdgcn_raw_buffer_store_b64(gr, rws, WS_X3 + (((s * 5 + 4) * 32 + h) * 16 + lane) * 8, 0, AUX_SC1);
    }
  }
  STAMP(8);
  if (h >= 5) return;
  {
    const int x3off = WS_X3 + (((s * 5 + h) * 32 + (lane >> 3)) * 16 + 2 * (lane & 7)) * 8;
    u32x4 gy[4];
    for (u32 spin = 0; spin < SPIN_CAP; ++spin) {
      u32 bad = 0;
      asm volatile("" ::: "memory");
#pragma unroll
      for (int i = 0; i < 4; ++i) gy[i] = __builtin_amdgcn_raw_buffer_load_b128(rws, x3off + i * 1024, 0, AUX_SC1);
#pragma unroll
      for (int i = 0; i < 4; ++i) bad |= gbad(gy[i], tag);
      if (__builtin_amdgcn_ballot_w64(bad != 0) == 0) break;
    }
    float y0 = (gval(gy[0].x) + gval(gy[1].x)) + (gval(gy[2].x) + gval(gy[3].x));
    float y1 = (gval(gy[0].z) + gval(gy[1].z)) + (gval(gy[2].z) + gval(gy[3].z));
    y0 += dpp<ROR8>(y0); y1 += dpp<ROR8>(y1);
    y0 = xrow_sum(y0); y1 = xrow_sum(y1);
    if (lane < 8) ((GAS u32*)a.out)[40 * s + 8 * h + lane] = pack2(lo16(xo) + y0, hi16(xo) + y1);
    if (b == 0 && lane == 0) {
      const u64 full = ((((u64)ep.y << 32) | ep.x) + 1);
      __hip_atomic_store((GAS u64*)(a.ws + WS_EPOCH), full, __ATOMIC_RELAXED, __HIP_MEMORY_SCOPE_AGENT);
    }
  }
  STAMP(9);
}

}

extern "C" void kernel_launch(void* const* d_in, const int* in_sizes, int n_in, void* d_out, int out_size, void* d_ws, size_t ws_size, hipStream_t stream) {
  fused::Args a;
  a.x = (const unsigned short*)d_in[0];
  a.K = (const unsigned short*)d_in[1];
  a.V = (const unsigned short*)d_in[2];
  a.wq = (const unsigned short*)d_in[3];
  a.wo = (const unsigned short*)d_in[4];
  a.w1 = (const unsigned short*)d_in[6];
  a.w3 = (const unsigned short*)d_in[7];
  a.w2 = (const unsigned short*)d_in[8];
  a.out = (unsigned short*)d_out;
  a.ws = (unsigned char*)d_ws;
  hipLaunchKernelGGL(fused::fused_kernel, dim3(fused::NWG), dim3(fused::NT), fused::L_END, stream, a);
  hipLaunchKernelGGL(fused::fused_kernel, dim3(fused::NWG), dim3(fused::NT), fused::L_END, stream, a);
}
```

```cpp
#include <hip/hip_runtime.h>
#include <stdint.h>

namespace fused {

typedef unsigned u32;
typedef unsigned long long u64;
typedef u32 u32x2 __attribute__((ext_vector_type(2)));
typedef u32 u32x4 __attribute__((ext_vector_type(4)));
typedef float f32x2 __attribute__((ext_vector_type(2)));
typedef float f32x4 __attribute__((ext_vector_type(4)));
typedef short s16x4 __attribute__((ext_vector_type(4)));
typedef short s16x8 __attribute__((ext_vector_type(8)));
typedef __bf16 bf16x2 __attribute__((ext_vector_type(2)));
typedef __bf16 bf16x8 __attribute__((ext_vector_type(8)));
#define LAS __attribute__((address_space(3)))
#define GAS __attribute__((address_space(1)))

constexpr int D = 640, H = 32, S = 8192, DH = 128, F = 1728, HD = H * DH;
constexpr int NWG = 256, NT = 512;
constexpr int NG = F / 64;
constexpr int NP = 32;
#define G0 4
#define GP 4
#define GPOLL0 2
constexpr int NGRP = (NP - G0 + GP - 1) / GP;
constexpr u32 SPIN_CAP = 1u << 18;
constexpr int AUX_NT = 2, AUX_SC1 = 16, RSRC_W3 = 0x00020000;

constexpr int WS_EPOCH = 0;
constexpr int WS_X1 = 4096;
constexpr int WS_X2 = WS_X1 + NWG * 256 * 8;
constexpr int WS_ML = WS_X2 + NWG * 128 * 8;
constexpr int WS_X3 = WS_ML + NWG * 128;
constexpr int WS_END = WS_X3 + 8 * 5 * 32 * 16 * 8;
constexpr int WS_STAMP = 2 << 20;

constexpr int L_WO = 0;
constexpr int L_W2 = 128 * 160;
constexpr int L_PART = L_W2 + 64 * 160;
constexpr int L_OP = L_PART + 8192;
constexpr int L_Q = L_OP + 4096;
constexpr int L_HID = L_Q + 256;
constexpr int L_MW = L_HID + 128;
constexpr int L_LW = L_MW + 32;
constexpr int L_FLAG = L_LW + 32;
constexpr int L_END = L_FLAG + 16;

struct Args {
  const unsigned short *x, *K, *V, *wq, *wo, *w1, *w3, *w2;
  unsigned short* out;
  unsigned char* ws;
};

__device__ __forceinline__ float asf(u32 u) { return __builtin_bit_cast(float, u); }
__device__ __forceinline__ u32 asu(float f) { return __builtin_bit_cast(u32, f); }
__device__ __forceinline__ float lo16(u32 u) { return asf(u << 16); }
__device__ __forceinline__ float hi16(u32 u) { return asf(u & 0xffff0000u); }
__device__ __forceinline__ u32 pack2(float a, float b) { f32x2 v = {a, b}; return __builtin_bit_cast(u32, __builtin_convertvector(v, bf16x2)); }
__device__ __forceinline__ float rbf(float a) { return (float)(__bf16)a; }
__device__ __forceinline__ float dot2(u32 a, u32 b, float c) {
  return __builtin_amdgcn_fdot2_f32_bf16(__builtin_bit_cast(bf16x2, a), __builtin_bit_cast(bf16x2, b), c, false);
}
template <int CTRL> __device__ __forceinline__ float dpp(float x) {
  return asf((u32)__builtin_amdgcn_mov_dpp((int)asu(x), CTRL, 0xf, 0xf, true));
}
constexpr int ROR1 = 0x121, ROR2 = 0x122, ROR4 = 0x124, ROR8 = 0x128;
__device__ __forceinline__ float row_sum(float x) { x += dpp<ROR8>(x); x += dpp<ROR4>(x); x += dpp<ROR2>(x); x += dpp<ROR1>(x); return x; }
__device__ __forceinline__ float swap16_sum(float x) { auto s = __builtin_amdgcn_permlane16_swap(asu(x), asu(x), false, false); return asf(s[0]) + asf(s[1]); }
__device__ __forceinline__ float swap32_sum(float x) { auto s = __builtin_amdgcn_permlane32_swap(asu(x), asu(x), false, false); return asf(s[0]) + asf(s[1]); }
__device__ __forceinline__ float xrow_sum(float x) { return swap32_sum(swap16_sum(x)); }
__device__ __forceinline__ float xrow_max(float x) {
  auto s = __builtin_amdgcn_permlane16_swap(asu(x), asu(x), false, false); x = fmaxf(asf(s[0]), asf(s[1]));
  auto t = __builtin_amdgcn_permlane32_swap(asu(x), asu(x), false, false); return fmaxf(asf(t[0]), asf(t[1]));
}
__device__ __forceinline__ float wave_sum(float x) { return xrow_sum(row_sum(x)); }
__device__ __forceinline__ f32x4 mfma(u32x4 a, u32x4 b, f32x4 c) {
  return __builtin_amdgcn_mfma_f32_16x16x32_bf16(__builtin_bit_cast(bf16x8, a), __builtin_bit_cast(bf16x8, b), c, 0, 0, 0);
}
__device__ __forceinline__ void lds_barrier() {
  asm volatile("s_waitcnt lgkmcnt(0)" ::: "memory");
  __builtin_amdgcn_s_barrier();
  asm volatile("" ::: "memory");
}
#define FENCE() do { asm volatile("" ::: "memory"); __builtin_amdgcn_sched_barrier(0); } while (0)

struct Tag { u32 lo, hi; };
__device__ __forceinline__ u32x4 gpack(float a, float b, Tag t) {
  u32x4 r; r.x = ((asu(a) + 0x80u) >> 8) | t.lo; r.y = t.hi; r.z = ((asu(b) + 0x80u) >> 8) | t.lo; r.w = t.hi; return r;
}
__device__ __forceinline__ u32 gbad(u32x4 g, Tag t) { return (((g.x ^ t.lo) | (g.z ^ t.lo)) >> 24) | (g.y ^ t.hi) | (g.w ^ t.hi); }
__device__ __forceinline__ float gval(u32 w) { return asf(w << 8); }

#define STAMP(i) do { } while (0)

__device__ __forceinline__ u32x4 bfrag(LAS unsigned char* img, int lane, int k0, int n0) {
  const int g = lane >> 4, q = (lane >> 2) & 3, p = lane & 3;
  LAS unsigned char* ad = img + (k0 + 8 * g + q) * 160 + (n0 + 4 * p) * 2;
  s16x4 t0 = __builtin_amdgcn_ds_read_tr16_b64_v4i16((LAS s16x4*)ad);
  s16x4 t1 = __builtin_amdgcn_ds_read_tr16_b64_v4i16((LAS s16x4*)(ad + 4 * 160));
  s16x8 r = __builtin_shufflevector(t0, t1, 0, 1, 2, 3, 4, 5, 6, 7);
  return __builtin_bit_cast(u32x4, r);
}

__global__ void __launch_bounds__(NT, 2) fused_kernel(Args a) {
  extern __shared__ __attribute__((aligned(16))) unsigned char smem[];
  LAS unsigned char* L = (LAS unsigned char*)smem;
  const int tid = threadIdx.x, lane = tid & 63, wave = __builtin_amdgcn_readfirstlane(tid >> 6);
  const int b = blockIdx.x, h = b >> 3, s = b & 7;
  const bool mlp = h < NG;
  asm volatile("" :: "s"(a.x), "s"(a.K), "s"(a.V), "s"(a.wq), "s"(a.wo), "s"(a.w1), "s"(a.w3), "s"(a.w2), "s"(a.out), "s"(a.ws));
  STAMP(0);

  const __amdgpu_buffer_rsrc_t rws = __builtin_amdgcn_make_buffer_rsrc((void*)a.ws, 0, WS_END, RSRC_W3);
  const __amdgpu_buffer_rsrc_t rk = __builtin_amdgcn_make_buffer_rsrc((void*)a.K, 0, H * S * DH * 2, RSRC_W3);
  const __amdgpu_buffer_rsrc_t rv = __builtin_amdgcn_make_buffer_rsrc((void*)a.V, 0, H * S * DH * 2, RSRC_W3);

  const u32x2 ep = __builtin_amdgcn_raw_buffer_load_b64(rws, WS_EPOCH, 0, AUX_SC1);
  u32 xs[5], xr[5];
#pragma unroll
  for (int i = 0; i < 5; ++i) xs[i] = ((const GAS u32*)a.x)[lane + 64 * i];
  const int prow = 80 * s + (tid >> 5), c32 = tid & 31;
#pragma unroll
  for (int i = 0; i < 5; ++i) xr[i] = ((const GAS unsigned short*)a.x)[prow + 16 * i];
  u32x4 wv[5];
  {
    const bool isq = c32 < 16 || !mlp;
    const GAS unsigned short* base = isq ? (const GAS unsigned short*)a.wq + h * DH + 8 * (c32 & 15)
                                         : (c32 < 24 ? (const GAS unsigned short*)a.w1 + 64 * h + 8 * (c32 - 16) : (const GAS unsigned short*)a.w3 + 64 * h + 8 * (c32 - 24));
    const int stride = isq ? HD : F;
    base += (size_t)prow * stride;
#pragma unroll
    for (int i = 0; i < 5; ++i) wv[i] = __builtin_nontemporal_load((const GAS u32x4*)(base + 16 * i * stride));
  }
  u32x4 wt[4];
#pragma unroll
  for (int j = 0; j < 4; ++j) {
    const int it = tid + 512 * j;
    const bool isw2 = it >= 1280 && it < 1920 && mlp;
    const int i2 = it < 1280 ? it : it - 1280;
    const int row = i2 / 10, ch = i2 - row * 10;
    const GAS unsigned short* p = isw2 ? (const GAS unsigned short*)a.w2 + (size_t)(64 * h + row) * D : (const GAS unsigned short*)a.wo + (size_t)(DH * h + row) * D;
    wt[j] = __builtin_nontemporal_load((const GAS u32x4*)(p + 80 * s + 8 * ch));
  }
  const u32 xo = ((const GAS u32*)a.x)[40 * s + 8 * (h < 5 ? h : 0) + (lane & 7)];
  FENCE();

  const int piece0 = h * 2048 + s * 256 + wave * NP;
  const int j0 = (h + 3 * s + 5 * wave) & (NP - 1);
  const int voff = lane * 16;
  u32x4 kv[NP];
#define POFF(i) ((piece0 + ((j0 + (i)) & (NP - 1))) << 10)
#pragma unroll
  for (int i = 0; i < G0; ++i) kv[i] = __builtin_amdgcn_raw_buffer_load_b128(rk, voff, POFF(i), AUX_NT);
  FENCE();

  float xn[5];
  {
    float sum = 0.f;
#pragma unroll
    for (int i = 0; i < 5; ++i) sum += lo16(xs[i]) + hi16(xs[i]);
    const float mean = wave_sum(sum) * (1.f / D);
    float sq = 0.f;
#pragma unroll
    for (int i = 0; i < 5; ++i) { const float d0 = lo16(xs[i]) - mean, d1 = hi16(xs[i]) - mean; sq += d0 * d0 + d1 * d1; }
    const float var = wave_sum(sq) * (1.f / D);
    const float mb = rbf(mean), sd = rbf(sqrtf(rbf(rbf(var) + 1e-5f)));
#pragma unroll
    for (int i = 0; i < 5; ++i) xn[i] = rbf(rbf(lo16(xr[i]) - mb) / sd);
  }
  {
    float acc[8];
#pragma unroll
    for (int j = 0; j < 8; ++j) acc[j] = 0.f;
#pragma unroll
    for (int i = 0; i < 5; ++i) {
      const u32 ph = asu(xn[i]), pl = ph >> 16;
#pragma unroll
      for (int j = 0; j < 4; ++j) { acc[2 * j] = dot2(wv[i][j], pl, acc[2 * j]); acc[2 * j + 1] = dot2(wv[i][j], ph, acc[2 * j + 1]); }
    }
#pragma unroll
    for (int j = 0; j < 8; ++j) acc[j] = swap32_sum(acc[j]);
    const bool up = lane >= 32;
    f32x4 o4 = {up ? acc[4] : acc[0], up ? acc[5] : acc[1], up ? acc[6] : acc[2], up ? acc[7] : acc[3]};
    *(LAS f32x4*)(L + L_PART + (wave * 256 + 8 * (lane & 31) + (up ? 4 : 0)) * 4) = o4;
    if (tid == 0) *(volatile LAS u32*)(L + L_FLAG) = 0u;
  }
  STAMP(1);
  lds_barrier();
  Tag tag;
  {
    const u32 e0 = __builtin_amdgcn_readfirstlane(ep.x), e1 = __builtin_amdgcn_readfirstlane(ep.y);
    const u64 e = (((u64)e1 << 32) | e0) + 1;
    tag.lo = (u32)(e & 0xff) << 24; tag.hi = (u32)(e >> 8);
  }
  if (wave == 0 || (wave == 1 && mlp)) {
    f32x2 sum = {0.f, 0.f};
#pragma unroll
    for (int w = 0; w < 8; ++w) sum += *(LAS f32x2*)(L + L_PART + (w * 256 + 2 * tid) * 4);
    __builtin_amdgcn_raw_buffer_store_b128(gpack(sum.x, sum.y, tag), rws, WS_X1 + (b * 256 + 2 * tid) * 8, 0, AUX_SC1);
  }
  STAMP(2);
  FENCE();

  bool mydone = !(wave == 0 || (wave == 1 && mlp));
  bool qready = false;
  const int x1off = WS_X1 + (h * 8 * 256 + wave * 128 + 2 * lane) * 8;
  auto finish_gather = [&](const u32x4 (&pq)[8]) {
    float s0 = 0.f, s1 = 0.f;
#pragma unroll
    for (int z = 0; z < 8; ++z) { s0 += gval(pq[z].x); s1 += gval(pq[z].z); }
    if (wave == 0) {
      *(volatile LAS u32*)(L + L_Q + lane * 4) = pack2(s0, s1);
      asm volatile("s_waitcnt lgkmcnt(0)" ::: "memory");
      *(volatile LAS u32*)(L + L_FLAG) = 1u;
    } else {
      const float g0 = rbf(s0), g1 = rbf(s1);
      auto t0 = __builtin_amdgcn_permlane32_swap(asu(g0), asu(g0), false, false);
      auto t1 = __builtin_amdgcn_permlane32_swap(asu(g1), asu(g1), false, false);
      const float ga = asf(t0[0]), ua = asf(t0[1]), gb = asf(t1[0]), ub = asf(t1[1]);
      const float ha = rbf(ga / (1.f + __expf(-ga))) * ua, hb = rbf(gb / (1.f + __expf(-gb))) * ub;
      if (lane < 32) *(volatile LAS u32*)(L + L_HID + lane * 4) = pack2(ha, hb);
    }
  };
#pragma unroll
  for (int g = 1; g <= NGRP; ++g) {
    const bool poll = !mydone && g >= GPOLL0;
    u32x4 pq[8];
    if (poll) {
#pragma unroll
      for (int z = 0; z < 8; ++z) pq[z] = __builtin_amdgcn_raw_buffer_load_b128(rws, x1off + z * 2048, 0, AUX_SC1);
    }
    FENCE();
#pragma unroll
    for (int i = G0 + (g - 1) * GP; i < G0 + g * GP && i < NP; ++i) kv[i] = __builtin_amdgcn_raw_buffer_load_b128(rk, voff, POFF(i), AUX_NT);
    FENCE();
    if (g == 1) {
#pragma unroll
      for (int j = 0; j < 4; ++j) if (tid + 512 * j < 1920) *(LAS u32x4*)(L + L_WO + (tid + 512 * j) * 16) = wt[j];
    }
    if (poll) {
      u32 bad = 0;
#pragma unroll
      for (int z = 0; z < 8; ++z) bad |= gbad(pq[z], tag);
      if (__builtin_amdgcn_ballot_w64(bad != 0) == 0) { finish_gather(pq); mydone = true; STAMP(10); }
    } else if (!qready) {
      asm volatile("s_waitcnt vmcnt(%0)" :: "n"(GP) : "memory");
    }
    if (!qready) qready = __builtin_amdgcn_readfirstlane(*(volatile LAS u32*)(L + L_FLAG)) != 0;
    FENCE();
  }
  if (!mydone) {
    for (u32 spin = 0; spin < SPIN_CAP; ++spin) {
      u32x4 pq[8];
      asm volatile("" ::: "memory");
#pragma unroll
      for (int z = 0; z < 8; ++z) pq[z] = __builtin_amdgcn_raw_buffer_load_b128(rws, x1off + z * 2048, 0, AUX_SC1);
      u32 bad = 0;
#pragma unroll
      for (int z = 0; z < 8; ++z) bad |= gbad(pq[z], tag);
      if (__builtin_amdgcn_ballot_w64(bad != 0) == 0) { finish_gather(pq); break; }
    }
  }
  if (!qready) {
    for (u32 spin = 0; spin < (SPIN_CAP << 4); ++spin) {
      if (__builtin_amdgcn_readfirstlane(*(volatile LAS u32*)(L + L_FLAG)) != 0) break;
      __builtin_amdgcn_s_sleep(1);
    }
  }
  asm volatile("" ::: "memory");
  const u32x4 qv = *(LAS u32x4*)(L + L_Q + (lane & 15) * 16);
  STAMP(3);
  FENCE();

  float sc[NP];
#pragma unroll
  for (int i = 0; i < NP; ++i) {
    float d = dot2(kv[i].x, qv.x, 0.f);
    d = dot2(kv[i].y, qv.y, d); d = dot2(kv[i].z, qv.z, d); d = dot2(kv[i].w, qv.w, d);
    kv[i] = __builtin_amdgcn_raw_buffer_load_b128(rv, voff, POFF(i), AUX_NT);
    d = row_sum(d);
    sc[i] = rbf(rbf(d) * 0.08838834764831845f);
    if ((i & 1) == 1) FENCE();
  }
  float mw = sc[0];
#pragma unroll
  for (int i = 1; i < NP; ++i) mw = fmaxf(mw, sc[i]);
  mw = xrow_max(mw);
  if (lane == 0) *(LAS float*)(L + L_MW + wave * 4) = mw;
  STAMP(4);
  lds_barrier();
  float M;
  {
    const f32x4 m0 = *(LAS f32x4*)(L + L_MW), m1 = *(LAS f32x4*)(L + L_MW + 16);
    M = fmaxf(fmaxf(fmaxf(m0.x, m0.y), fmaxf(m0.z, m0.w)), fmaxf(fmaxf(m1.x, m1.y), fmaxf(m1.z, m1.w)));
  }
  float lsum = 0.f;
#pragma unroll
  for (int i = 0; i < NP; ++i) {
    const float p = __builtin_amdgcn_exp2f((sc[i] - M) * 1.4426950408889634f);
    lsum += p;
    sc[i] = rbf(p);
  }
  lsum = xrow_sum(lsum);
  if (lane == 0) *(LAS float*)(L + L_LW + wave * 4) = lsum;
  FENCE();

  float acc[8];
#pragma unroll
  for (int j = 0; j < 8; ++j) acc[j] = 0.f;
#pragma unroll
  for (int i = 0; i < NP; ++i) {
    const u32 ph = asu(sc[i]), pl = ph >> 16;
#pragma unroll
    for (int j = 0; j < 4; ++j) { acc[2 * j] = dot2(kv[i][j], pl, acc[2 * j]); acc[2 * j + 1] = dot2(kv[i][j], ph, acc[2 * j + 1]); }
    if ((i & 1) == 1) FENCE();
  }
#pragma unroll
  for (int j = 0; j < 8; ++j) acc[j] = xrow_sum(acc[j]);
  if (lane < 32) {
    const bool up = lane >= 16;
    f32x4 o4 = {up ? acc[4] : acc[0], up ? acc[5] : acc[1], up ? acc[6] : acc[2], up ? acc[7] : acc[3]};
    *(LAS f32x4*)(L + L_OP + (wave * 128 + 8 * (lane & 15) + (up ? 4 : 0)) * 4) = o4;
  }
  STAMP(5);
  lds_barrier();
  if (wave != 0) return;

  {
    f32x2 o2 = {0.f, 0.f};
#pragma unroll
    for (int w = 0; w < 8; ++w) o2 += *(LAS f32x2*)(L + L_OP + (w * 128 + 2 * lane) * 4);
    __builtin_amdgcn_raw_buffer_store_b128(gpack(o2.x, o2.y, tag), rws, WS_X2 + (b * 128 + 2 * lane) * 8, 0, AUX_SC1);
    const f32x4 l0 = *(LAS f32x4*)(L + L_LW), l1 = *(LAS f32x4*)(L + L_LW + 16);
    const float lw = ((l0.x + l0.y) + (l0.z + l0.w)) + ((l1.x + l1.y) + (l1.z + l1.w));
    if (lane == 0) __builtin_amdgcn_raw_buffer_store_b128(gpack(M, lw, tag), rws, WS_ML + b * 128, 0, AUX_SC1);
  }
  STAMP(6);
  const int pz = lane & 7, half = (lane >> 3) & 1, g4 = lane >> 4;
  const int x2off = WS_X2 + ((h * 8 + pz) * 128 + 64 * half + 8 * g4) * 8;
  const int mloff = WS_ML + (h * 8 + pz) * 128;
  u32x4 ga[8], gm;
#define HOP1_LOADS() do { \
    _Pragma("unroll") for (int kk = 0; kk < 2; ++kk) \
      _Pragma("unroll") for (int u = 0; u < 4; ++u) ga[kk * 4 + u] = __builtin_amdgcn_raw_buffer_load_b128(rws, x2off + kk * 256 + u * 16, 0, AUX_SC1); \
    gm = __builtin_amdgcn_raw_buffer_load_b128(rws, mloff, 0, AUX_SC1); } while (0)
  HOP1_LOADS();
  FENCE();
  float yb[5];
  {
    if (mlp) {
      const u32x4 ha0 = *(LAS u32x4*)(L + L_HID + g4 * 16), ha1 = *(LAS u32x4*)(L + L_HID + 64 + g4 * 16);
#pragma unroll
      for (int t = 0; t < 5; ++t) {
        f32x4 d = {0.f, 0.f, 0.f, 0.f};
        d = mfma(ha0, bfrag(L + L_W2, lane, 0, 16 * t), d);
        d = mfma(ha1, bfrag(L + L_W2, lane, 32, 16 * t), d);
        yb[t] = d[0];
      }
    } else {
#pragma unroll
      for (int t = 0; t < 5; ++t) yb[t] = 0.f;
    }
  }
  u32x4 bf[5][4];
#pragma unroll
  for (int t = 0; t < 5; ++t)
#pragma unroll
    for (int ks = 0; ks < 4; ++ks) bf[t][ks] = bfrag(L + L_WO, lane, 32 * ks, 16 * t);
  FENCE();
  for (u32 spin = 0; spin < SPIN_CAP; ++spin) {
    u32 bad = gbad(gm, tag);
#pragma unroll
    for (int u = 0; u < 8; ++u) bad |= gbad(ga[u], tag);
    if (__builtin_amdgcn_ballot_w64(bad != 0) == 0) break;
    asm volatile("" ::: "memory");
    HOP1_LOADS();
  }
  STAMP(7);
  {
    const float mz = gval(gm.x), lz = gval(gm.z);
    float mx = mz;
    mx = fmaxf(mx, dpp<0xB1>(mx)); mx = fmaxf(mx, dpp<0x4E>(mx)); mx = fmaxf(mx, dpp<0x141>(mx));
    const float e = __builtin_amdgcn_exp2f((mz - mx) * 1.4426950408889634f);
    float ls = e * lz;
    ls += dpp<0xB1>(ls); ls += dpp<0x4E>(ls); ls += dpp<0x141>(ls);
    const float wz = e / ls;
    u32x4 af[2];
#pragma unroll
    for (int kk = 0; kk < 2; ++kk)
#pragma unroll
      for (int u = 0; u < 4; ++u) af[kk][u] = pack2(gval(ga[kk * 4 + u].x) * wz, gval(ga[kk * 4 + u].z) * wz);
    float y[5];
#pragma unroll
    for (int t = 0; t < 5; ++t) {
      f32x4 d1 = {0.f, 0.f, 0.f, 0.f}, d2 = {0.f, 0.f, 0.f, 0.f};
      d1 = mfma(af[0], bf[t][0], d1); d2 = mfma(af[0], bf[t][2], d2);
      d1 = mfma(af[1], bf[t][1], d1); d2 = mfma(af[1], bf[t][3], d2);
      const float u1 = (d1[0] + d1[1]) + (d1[2] + d1[3]), u2 = (d2[0] + d2[1]) + (d2[2] + d2[3]);
      y[t] = xrow_sum(lane < 32 ? u1 : u2) + yb[t];
    }
    const float ysel = g4 == 0 ? y[0] : g4 == 1 ? y[1] : g4 == 2 ? y[2] : y[3];
    const u32 thi = tag.hi;
    u32x2 gr; gr.x = ((asu(ysel) + 0x80u) >> 8) | tag.lo; gr.y = thi;
    __builtin_amdgcn_raw_buffer_store_b64(gr, rws, WS_X3 + (((s * 5 + g4) * 32 + h) * 16 + (lane & 15)) * 8, 0, AUX_SC1);
    if (lane < 16) {
      gr.x = ((asu(y[4]) + 0x80u) >> 8) | tag.lo;
      __builtin_amdgcn_raw_buffer_store_b64(gr, rws, WS_X3 + (((s * 5 + 4) * 32 + h) * 16 + lane) * 8, 0, AUX_SC1);
    }
  }
  STAMP(8);
  if (h >= 5) return;
  {
    const int x3off = WS_X3 + (((s * 5 + h) * 32 + (lane >> 3)) * 16 + 2 * (lane & 7)) * 8;
    u32x4 gy[4];
    for (u32 spin = 0; spin < SPIN_CAP; ++spin) {
      u32 bad = 0;
      asm volatile("" ::: "memory");
#pragma unroll
      for (int i = 0; i < 4; ++i) gy[i] = __builtin_amdgcn_raw_buffer_load_b128(rws, x3off + i * 1024, 0, AUX_SC1);
#pragma unroll
      for (int i = 0; i < 4; ++i) bad |= gbad(gy[i], tag);
      if (__builtin_amdgcn_ballot_w64(bad != 0) == 0) break;
    }
    float y0 = (gval(gy[0].x) + gval(gy[1].x)) + (gval(gy[2].x) + gval(gy[3].x));
    float y1 = (gval(gy[0].z) + gval(gy[1].z)) + (gval(gy[2].z) + gval(gy[3].z));
    y0 += dpp<ROR8>(y0); y1 += dpp<ROR8>(y1);
    y0 = xrow_sum(y0); y1 = xrow_sum(y1);
    if (lane < 8) ((GAS u32*)a.out)[40 * s + 8 * h + lane] = pack2(lo16(xo) + y0, hi16(xo) + y1);
    if (b == 0 && lane == 0) {
      const u64 full = ((((u64)ep.y << 32) | ep.x) + 1);
      __hip_atomic_store((GAS u64*)(a.ws + WS_EPOCH), full, __ATOMIC_RELAXED, __HIP_MEMORY_SCOPE_AGENT);
    }
  }
  STAMP(9);
}

struct Touch { const unsigned char* p[10]; unsigned n[10]; unsigned stride; unsigned ticks; unsigned* sink; };
__global__ void touch_kernel(Touch t) {
  const unsigned long long t0 = __builtin_amdgcn_s_memrealtime();
  unsigned acc = 0;
  for (int i = 0; i < 10; ++i)
    for (unsigned off = threadIdx.x * t.stride; off < t.n[i]; off += blockDim.x * t.stride) acc += *(const volatile GAS unsigned*)(t.p[i] + off);
  if (acc == 0x9e3779b9u) *t.sink = acc;
  while (__builtin_amdgcn_s_memrealtime() - t0 < t.ticks) __builtin_amdgcn_s_sleep(1);
}
}

extern "C" void kernel_launch(void* const* d_in, const int* in_sizes, int n_in, void* d_out, int out_size, void* d_ws, size_t ws_size, hipStream_t stream) {
  fused::Args a;
  a.x = (const unsigned short*)d_in[0];
  a.K = (const unsigned short*)d_in[1];
  a.V = (const unsigned short*)d_in[2];
  a.wq = (const unsigned short*)d_in[3];
  a.wo = (const unsigned short*)d_in[4];
  a.w1 = (const unsigned short*)d_in[6];
  a.w3 = (const unsigned short*)d_in[7];
  a.w2 = (const unsigned short*)d_in[8];
  a.out = (unsigned short*)d_out;
  a.ws = (unsigned char*)d_ws;
  fused::Touch t;
  for (int i = 0; i < 9; ++i) { t.p[i] = (const unsigned char*)d_in[i]; t.n[i] = (true) ? (unsigned)in_sizes[i] * 2u - 4u : 0u; }
  t.p[9] = (const unsigned char*)d_ws; t.n[9] = (1u << 20); t.stride = 4096; t.ticks = 10000; t.sink = (unsigned*)((char*)d_ws + (8 << 20));
  hipLaunchKernelGGL(fused::touch_kernel, dim3(256), dim3(256), 0, stream, t);
  hipLaunchKernelGGL(fused::fused_kernel, dim3(fused::NWG), dim3(fused::NT), fused::L_END, stream, a);
}
```

```cpp
#include <hip/hip_runtime.h>
#include <stdint.h>

namespace fused {

typedef unsigned u32;
typedef unsigned long long u64;
typedef u32 u32x2 __attribute__((ext_vector_type(2)));
typedef u32 u32x4 __attribute__((ext_vector_type(4)));
typedef float f32x2 __attribute__((ext_vector_type(2)));
typedef float f32x4 __attribute__((ext_vector_type(4)));
typedef short s16x4 __attribute__((ext_vector_type(4)));
typedef short s16x8 __attribute__((ext_vector_type(8)));
typedef __bf16 bf16x2 __attribute__((ext_vector_type(2)));
typedef __bf16 bf16x8 __attribute__((ext_vector_type(8)));
#define LAS __attribute__((address_space(3)))
#define GAS __attribute__((address_space(1)))

constexpr int D = 640, H = 32, S = 8192, DH = 128, F = 1728, HD = H * DH;
constexpr int NWG = 256, NT = 512;
constexpr int NG = F / 64;
constexpr int NP = 32;
#define G0 4
#define GP 4
#define GPOLL0 2
constexpr int NGRP = (NP - G0 + GP - 1) / GP;
constexpr u32 SPIN_CAP = 1u << 18;
constexpr int AUX_NT = 2, AUX_SC1 = 16, RSRC_W3 = 0x00020000;

constexpr int WS_EPOCH = 0;
constexpr int WS_X1 = 4096;
constexpr int WS_X2 = WS_X1 + NWG * 256 * 8;
constexpr int WS_ML = WS_X2 + NWG * 128 * 8;
constexpr int WS_X3 = WS_ML + NWG * 128;
constexpr int WS_END = WS_X3 + 8 * 5 * 32 * 16 * 8;
constexpr int WS_STAMP = 2 << 20;

constexpr int L_WO = 0;
constexpr int L_W2 = 128 * 160;
constexpr int L_PART = L_W2 + 64 * 160;
constexpr int L_OP = L_PART + 8192;
constexpr int L_Q = L_OP + 4096;
constexpr int L_HID = L_Q + 256;
constexpr int L_MW = L_HID + 128;
constexpr int L_LW = L_MW + 32;
constexpr int L_FLAG = L_LW + 32;
constexpr int L_END = L_FLAG + 16;

struct Args {
  const unsigned short *x, *K, *V, *wq, *wo, *w1, *w3, *w2;
  unsigned short* out;
  unsigned char* ws;
};

__device__ __forceinline__ float asf(u32 u) { return __builtin_bit_cast(float, u); }
__device__ __forceinline__ u32 asu(float f) { return __builtin_bit_cast(u32, f); }
__device__ __forceinline__ float lo16(u32 u) { return asf(u << 16); }
__device__ __forceinline__ float hi16(u32 u) { return asf(u & 0xffff0000u); }
__device__ __forceinline__ u32 pack2(float a, float b) { f32x2 v = {a, b}; return __builtin_bit_cast(u32, __builtin_convertvector(v, bf16x2)); }
__device__ __forceinline__ float rbf(float a) { return (float)(__bf16)a; }
__device__ __forceinline__ float dot2(u32 a, u32 b, float c) {
  return __builtin_amdgcn_fdot2_f32_bf16(__builtin_bit_cast(bf16x2, a), __builtin_bit_cast(bf16x2, b), c, false);
}
template <int CTRL> __device__ __forceinline__ float dpp(float x) {
  return asf((u32)__builtin_amdgcn_mov_dpp((int)asu(x), CTRL, 0xf, 0xf, true));
}
constexpr int ROR1 = 0x121, ROR2 = 0x122, ROR4 = 0x124, ROR8 = 0x128;
__device__ __forceinline__ float row_sum(float x) { x += dpp<ROR8>(x); x += dpp<ROR4>(x); x += dpp<ROR2>(x); x += dpp<ROR1>(x); return x; }
__device__ __forceinline__ float swap16_sum(float x) { auto s = __builtin_amdgcn_permlane16_swap(asu(x), asu(x), false, false); return asf(s[0]) + asf(s[1]); }
__device__ __forceinline__ float swap32_sum(float x) { auto s = __builtin_amdgcn_permlane32_swap(asu(x), asu(x), false, false); return asf(s[0]) + asf(s[1]); }
__device__ __forceinline__ float xrow_sum(float x) { return swap32_sum(swap16_sum(x)); }
__device__ __forceinline__ float xrow_max(float x) {
  auto s = __builtin_amdgcn_permlane16_swap(asu(x), asu(x), false, false); x = fmaxf(asf(s[0]), asf(s[1]));
  auto t = __builtin_amdgcn_permlane32_swap(asu(x), asu(x), false, false); return fmaxf(asf(t[0]), asf(t[1]));
}
__device__ __forceinline__ float wave_sum(float x) { return xrow_sum(row_sum(x)); }
__device__ __forceinline__ f32x4 mfma(u32x4 a, u32x4 b, f32x4 c) {
  return __builtin_amdgcn_mfma_f32_16x16x32_bf16(__builtin_bit_cast(bf16x8, a), __builtin_bit_cast(bf16x8, b), c, 0, 0, 0);
}
__device__ __forceinline__ void lds_barrier() {
  asm volatile("s_waitcnt lgkmcnt(0)" ::: "memory");
  __builtin_amdgcn_s_barrier();
  asm volatile("" ::: "memory");
}
#define FENCE() do { asm volatile("" ::: "memory"); __builtin_amdgcn_sched_barrier(0); } while (0)

struct Tag { u32 lo, hi; };
__device__ __forceinline__ u32x4 gpack(float a, float b, Tag t) {
  u32x4 r; r.x = ((asu(a) + 0x80u) >> 8) | t.lo; r.y = t.hi; r.z = ((asu(b) + 0x80u) >> 8) | t.lo; r.w = t.hi; return r;
}
__device__ __forceinline__ u32 gbad(u32x4 g, Tag t) { return (((g.x ^ t.lo) | (g.z ^ t.lo)) >> 24) | (g.y ^ t.hi) | (g.w ^ t.hi); }
__device__ __forceinline__ float gval(u32 w) { return asf(w << 8); }

#define STAMP(i) do { } while (0)

__device__ __forceinline__ u32x4 bfrag(LAS unsigned char* img, int lane, int k0, int n0) {
  const int g = lane >> 4, q = (lane >> 2) & 3, p = lane & 3;
  LAS unsigned char* ad = img + (k0 + 8 * g + q) * 160 + (n0 + 4 * p) * 2;
  s16x4 t0 = __builtin_amdgcn_ds_read_tr16_b64_v4i16((LAS s16x4*)ad);
  s16x4 t1 = __builtin_amdgcn_ds_read_tr16_b64_v4i16((LAS s16x4*)(ad + 4 * 160));
  s16x8 r = __builtin_shufflevector(t0, t1, 0, 1, 2, 3, 4, 5, 6, 7);
  return __builtin_bit_cast(u32x4, r);
}

__global__ void __launch_bounds__(NT, 2) fused_kernel(Args a) {
  extern __shared__ __attribute__((aligned(16))) unsigned char smem[];
  LAS unsigned char* L = (LAS unsigned char*)smem;
  const int tid = threadIdx.x, lane = tid & 63, wave = __builtin_amdgcn_readfirstlane(tid >> 6);
  const int b = blockIdx.x, h = b >> 3, s = b & 7;
  const bool mlp = h < NG;
  asm volatile("" :: "s"(a.x), "s"(a.K), "s"(a.V), "s"(a.wq), "s"(a.wo), "s"(a.w1), "s"(a.w3), "s"(a.w2), "s"(a.out), "s"(a.ws));
  STAMP(0);

  const __amdgpu_buffer_rsrc_t rws = __builtin_amdgcn_make_buffer_rsrc((void*)a.ws, 0, WS_END, RSRC_W3);
  const __amdgpu_buffer_rsrc_t rk = __builtin_amdgcn_make_buffer_rsrc((void*)a.K, 0, H * S * DH * 2, RSRC_W3);
  const __amdgpu_buffer_rsrc_t rv = __builtin_amdgcn_make_buffer_rsrc((void*)a.V, 0, H * S * DH * 2, RSRC_W3);

  const u32x2 ep = __builtin_amdgcn_raw_buffer_load_b64(rws, WS_EPOCH, 0, AUX_SC1);
  u32 xs[5], xr[5];
#pragma unroll
  for (int i = 0; i < 5; ++i) xs[i] = ((const GAS u32*)a.x)[lane + 64 * i];
  const int prow = 80 * s + (tid >> 5), c32 = tid & 31;
#pragma unroll
  for (int i = 0; i < 5; ++i) xr[i] = ((const GAS unsigned short*)a.x)[prow + 16 * i];
  u32x4 wv[5];
  {
    const bool isq = c32 < 16 || !mlp;
    const GAS unsigned short* base = isq ? (const GAS unsigned short*)a.wq + h * DH + 8 * (c32 & 15)
                                         : (c32 < 24 ? (const GAS unsigned short*)a.w1 + 64 * h + 8 * (c32 - 16) : (const GAS unsigned short*)a.w3 + 64 * h + 8 * (c32 - 24));
    const int stride = isq ? HD : F;
    base += (size_t)prow * stride;
#pragma unroll
    for (int i = 0; i < 5; ++i) wv[i] = __builtin_nontemporal_load((const GAS u32x4*)(base + 16 * i * stride));
  }
  u32x4 wt[4];
#pragma unroll
  for (int j = 0; j < 4; ++j) {
    const int it = tid + 512 * j;
    const bool isw2 = it >= 1280 && it < 1920 && mlp;
    const int i2 = it < 1280 ? it : it - 1280;
    const int row = i2 / 10, ch = i2 - row * 10;
    const GAS unsigned short* p = isw2 ? (const GAS unsigned short*)a.w2 + (size_t)(64 * h + row) * D : (const GAS unsigned short*)a.wo + (size_t)(DH * h + row) * D;
    wt[j] = __builtin_nontemporal_load((const GAS u32x4*)(p + 80 * s + 8 * ch));
  }
  const u32 xo = ((const GAS u32*)a.x)[40 * s + 8 * (h < 5 ? h : 0) + (lane & 7)];
  FENCE();

  const int piece0 = h * 2048 + s * 256 + wave * NP;
  const int j0 = (h + 3 * s + 5 * wave) & (NP - 1);
  const int voff = lane * 16;
  u32x4 kv[NP];
#define POFF(i) ((piece0 + ((j0 + (i)) & (NP - 1))) << 10)
#pragma unroll
  for (int i = 0; i < G0; ++i) kv[i] = __builtin_amdgcn_raw_buffer_load_b128(rk, voff, POFF(i), AUX_NT);
  FENCE();

  float xn[5];
  {
    float sum = 0.f;
#pragma unroll
    for (int i = 0; i < 5; ++i) sum += lo16(xs[i]) + hi16(xs[i]);
    const float mean = wave_sum(sum) * (1.f / D);
    float sq = 0.f;
#pragma unroll
    for (int i = 0; i < 5; ++i) { const float d0 = lo16(xs[i]) - mean, d1 = hi16(xs[i]) - mean; sq += d0 * d0 + d1 * d1; }
    const float var = wave_sum(sq) * (1.f / D);
    const float mb = rbf(mean), sd = rbf(sqrtf(rbf(rbf(var) + 1e-5f)));
#pragma unroll
    for (int i = 0; i < 5; ++i) xn[i] = rbf(rbf(lo16(xr[i]) - mb) / sd);
  }
  {
    float acc[8];
#pragma unroll
    for (int j = 0; j < 8; ++j) acc[j] = 0.f;
#pragma unroll
    for (int i = 0; i < 5; ++i) {
      const u32 ph = asu(xn[i]), pl = ph >> 16;
#pragma unroll
      for (int j = 0; j < 4; ++j) { acc[2 * j] = dot2(wv[i][j], pl, acc[2 * j]); acc[2 * j + 1] = dot2(wv[i][j], ph, acc[2 * j + 1]); }
    }
#pragma unroll
    for (int j = 0; j < 8; ++j) acc[j] = swap32_sum(acc[j]);
    const bool up = lane >= 32;
    f32x4 o4 = {up ? acc[4] : acc[0], up ? acc[5] : acc[1], up ? acc[6] : acc[2], up ? acc[7] : acc[3]};
    *(LAS f32x4*)(L + L_PART + (wave * 256 + 8 * (lane & 31) + (up ? 4 : 0)) * 4) = o4;
    if (tid == 0) *(volatile LAS u32*)(L + L_FLAG) = 0u;
  }
  STAMP(1);
  lds_barrier();
  Tag tag;
  {
    const u32 e0 = __builtin_amdgcn_readfirstlane(ep.x), e1 = __builtin_amdgcn_readfirstlane(ep.y);
    const u64 e = (((u64)e1 << 32) | e0) + 1;
    tag.lo = (u32)(e & 0xff) << 24; tag.hi = (u32)(e >> 8);
  }
  if (wave == 0 || (wave == 1 && mlp)) {
    f32x2 sum = {0.f, 0.f};
#pragma unroll
    for (int w = 0; w < 8; ++w) sum += *(LAS f32x2*)(L + L_PART + (w * 256 + 2 * tid) * 4);
    __builtin_amdgcn_raw_buffer_store_b128(gpack(sum.x, sum.y, tag), rws, WS_X1 + (b * 256 + 2 * tid) * 8, 0, AUX_SC1);
  }
  STAMP(2);
  FENCE();

  bool mydone = !(wave == 0 || (wave == 1 && mlp));
  bool qready = false;
  const int x1off = WS_X1 + (h * 8 * 256 + wave * 128 + 2 * lane) * 8;
  auto finish_gather = [&](const u32x4 (&pq)[8]) {
    float s0 = 0.f, s1 = 0.f;
#pragma unroll
    for (int z = 0; z < 8; ++z) { s0 += gval(pq[z].x); s1 += gval(pq[z].z); }
    if (wave == 0) {
      *(volatile LAS u32*)(L + L_Q + lane * 4) = pack2(s0, s1);
      asm volatile("s_waitcnt lgkmcnt(0)" ::: "memory");
      *(volatile LAS u32*)(L + L_FLAG) = 1u;
    } else {
      const float g0 = rbf(s0), g1 = rbf(s1);
      auto t0 = __builtin_amdgcn_permlane32_swap(asu(g0), asu(g0), false, false);
      auto t1 = __builtin_amdgcn_permlane32_swap(asu(g1), asu(g1), false, false);
      const float ga = asf(t0[0]), ua = asf(t0[1]), gb = asf(t1[0]), ub = asf(t1[1]);
      const float ha = rbf(ga / (1.f + __expf(-ga))) * ua, hb = rbf(gb / (1.f + __expf(-gb))) * ub;
      if (lane < 32) *(volatile LAS u32*)(L + L_HID + lane * 4) = pack2(ha, hb);
    }
  };
#pragma unroll
  for (int g = 1; g <= NGRP; ++g) {
    const bool poll = !mydone && g >= GPOLL0;
    u32x4 pq[8];
    if (poll) {
#pragma unroll
      for (int z = 0; z < 8; ++z) pq[z] = __builtin_amdgcn_raw_buffer_load_b128(rws, x1off + z * 2048, 0, AUX_SC1);
    }
    FENCE();
#pragma unroll
    for (int i = G0 + (g - 1) * GP; i < G0 + g * GP && i < NP; ++i) kv[i] = __builtin_amdgcn_raw_buffer_load_b128(rk, voff, POFF(i), AUX_NT);
    FENCE();
    if (g == 1) {
#pragma unroll
      for (int j = 0; j < 4; ++j) if (tid + 512 * j < 1920) *(LAS u32x4*)(L + L_WO + (tid + 512 * j) * 16) = wt[j];
    }
    if (poll) {
      u32 bad = 0;
#pragma unroll
      for (int z = 0; z < 8; ++z) bad |= gbad(pq[z], tag);
      if (__builtin_amdgcn_ballot_w64(bad != 0) == 0) { finish_gather(pq); mydone = true; STAMP(10); }
    } else if (!qready) {
      asm volatile("s_waitcnt vmcnt(%0)" :: "n"(GP) : "memory");
    }
    if (!qready) qready = __builtin_amdgcn_readfirstlane(*(volatile LAS u32*)(L + L_FLAG)) != 0;
    FENCE();
  }
  if (!mydone) {
    for (u32 spin = 0; spin < SPIN_CAP; ++spin) {
      u32x4 pq[8];
      asm volatile("" ::: "memory");
#pragma unroll
      for (int z = 0; z < 8; ++z) pq[z] = __builtin_amdgcn_raw_buffer_load_b128(rws, x1off + z * 2048, 0, AUX_SC1);
      u32 bad = 0;
#pragma unroll
      for (int z = 0; z < 8; ++z) bad |= gbad(pq[z], tag);
      if (__builtin_amdgcn_ballot_w64(bad != 0) == 0) { finish_gather(pq); break; }
    }
  }
  if (!qready) {
    for (u32 spin = 0; spin < (SPIN_CAP << 4); ++spin) {
      if (__builtin_amdgcn_readfirstlane(*(volatile LAS u32*)(L + L_FLAG)) != 0) break;
      __builtin_amdgcn_s_sleep(1);
    }
  }
  asm volatile("" ::: "memory");
  const u32x4 qv = *(LAS u32x4*)(L + L_Q + (lane & 15) * 16);
  STAMP(3);
  FENCE();

  float sc[NP];
#pragma unroll
  for (int i = 0; i < NP; ++i) {
    float d = dot2(kv[i].x, qv.x, 0.f);
    d = dot2(kv[i].y, qv.y, d); d = dot2(kv[i].z, qv.z, d); d = dot2(kv[i].w, qv.w, d);
    kv[i] = __builtin_amdgcn_raw_buffer_load_b128(rv, voff, POFF(i), AUX_NT);
    d = row_sum(d);
    sc[i] = rbf(rbf(d) * 0.08838834764831845f);
    if ((i & 1) == 1) FENCE();
  }
  float mw = sc[0];
#pragma unroll
  for (int i = 1; i < NP; ++i) mw = fmaxf(mw, sc[i]);
  mw = xrow_max(mw);
  if (lane == 0) *(LAS float*)(L + L_MW + wave * 4) = mw;
  STAMP(4);
  lds_barrier();
  float M;
  {
    const f32x4 m0 = *(LAS f32x4*)(L + L_MW), m1 = *(LAS f32x4*)(L + L_MW + 16);
    M = fmaxf(fmaxf(fmaxf(m0.x, m0.y), fmaxf(m0.z, m0.w)), fmaxf(fmaxf(m1.x, m1.y), fmaxf(m1.z, m1.w)));
  }
  float lsum = 0.f;
#pragma unroll
  for (int i = 0; i < NP; ++i) {
    const float p = __builtin_amdgcn_exp2f((sc[i] - M) * 1.4426950408889634f);
    lsum += p;
    sc[i] = rbf(p);
  }
  lsum = xrow_sum(lsum);
  if (lane == 0) *(LAS float*)(L + L_LW + wave * 4) = lsum;
  FENCE();

  float acc[8];
#pragma unroll
  for (int j = 0; j < 8; ++j) acc[j] = 0.f;
#pragma unroll
  for (int i = 0; i < NP; ++i) {
    const u32 ph = asu(sc[i]), pl = ph >> 16;
#pragma unroll
    for (int j = 0; j < 4; ++j) { acc[2 * j] = dot2(kv[i][j], pl, acc[2 * j]); acc[2 * j + 1] = dot2(kv[i][j], ph, acc[2 * j + 1]); }
    if ((i & 1) == 1) FENCE();
  }
#pragma unroll
  for (int j = 0; j < 8; ++j) acc[j] = xrow_sum(acc[j]);
  if (lane < 32) {
    const bool up = lane >= 16;
    f32x4 o4 = {up ? acc[4] : acc[0], up ? acc[5] : acc[1], up ? acc[6] : acc[2], up ? acc[7] : acc[3]};
    *(LAS f32x4*)(L + L_OP + (wave * 128 + 8 * (lane & 15) + (up ? 4 : 0)) * 4) = o4;
  }
  STAMP(5);
  lds_barrier();
  if (wave != 0) return;

  {
    f32x2 o2 = {0.f, 0.f};
#pragma unroll
    for (int w = 0; w < 8; ++w) o2 += *(LAS f32x2*)(L + L_OP + (w * 128 + 2 * lane) * 4);
    __builtin_amdgcn_raw_buffer_store_b128(gpack(o2.x, o2.y, tag), rws, WS_X2 + (b * 128 + 2 * lane) * 8, 0, AUX_SC1);
    const f32x4 l0 = *(LAS f32x4*)(L + L_LW), l1 = *(LAS f32x4*)(L + L_LW + 16);
    const float lw = ((l0.x + l0.y) + (l0.z + l0.w)) + ((l1.x + l1.y) + (l1.z + l1.w));
    if (lane == 0) __builtin_amdgcn_raw_buffer_store_b128(gpack(M, lw, tag), rws, WS_ML + b * 128, 0, AUX_SC1);
  }
  STAMP(6);
  const int pz = lane & 7, half = (lane >> 3) & 1, g4 = lane >> 4;
  const int x2off = WS_X2 + ((h * 8 + pz) * 128 + 64 * half + 8 * g4) * 8;
  const int mloff = WS_ML + (h * 8 + pz) * 128;
  u32x4 ga[8], gm;
#define HOP1_LOADS() do { \
    _Pragma("unroll") for (int kk = 0; kk < 2; ++kk) \
      _Pragma("unroll") for (int u = 0; u < 4; ++u) ga[kk * 4 + u] = __builtin_amdgcn_raw_buffer_load_b128(rws, x2off + kk * 256 + u * 16, 0, AUX_SC1); \
    gm = __builtin_amdgcn_raw_buffer_load_b128(rws, mloff, 0, AUX_SC1); } while (0)
  HOP1_LOADS();
  FENCE();
  float yb[5];
  {
    if (mlp) {
      const u32x4 ha0 = *(LAS u32x4*)(L + L_HID + g4 * 16), ha1 = *(LAS u32x4*)(L + L_HID + 64 + g4 * 16);
#pragma unroll
      for (int t = 0; t < 5; ++t) {
        f32x4 d = {0.f, 0.f, 0.f, 0.f};
        d = mfma(ha0, bfrag(L + L_W2, lane, 0, 16 * t), d);
        d = mfma(ha1, bfrag(L + L_W2, lane, 32, 16 * t), d);
        yb[t] = d[0];
      }
    } else {
#pragma unroll
      for (int t = 0; t < 5; ++t) yb[t] = 0.f;
    }
  }
  u32x4 bf[5][4];
#pragma unroll
  for (int t = 0; t < 5; ++t)
#pragma unroll
    for (int ks = 0; ks < 4; ++ks) bf[t][ks] = bfrag(L + L_WO, lane, 32 * ks, 16 * t);
  FENCE();
  for (u32 spin = 0; spin < SPIN_CAP; ++spin) {
    u32 bad = gbad(gm, tag);
#pragma unroll
    for (int u = 0; u < 8; ++u) bad |= gbad(ga[u], tag);
    if (__builtin_amdgcn_ballot_w64(bad != 0) == 0) break;
    asm volatile("" ::: "memory");
    HOP1_LOADS();
  }
  STAMP(7);
  {
    const float mz = gval(gm.x), lz = gval(gm.z);
    float mx = mz;
    mx = fmaxf(mx, dpp<0xB1>(mx)); mx = fmaxf(mx, dpp<0x4E>(mx)); mx = fmaxf(mx, dpp<0x141>(mx));
    const float e = __builtin_amdgcn_exp2f((mz - mx) * 1.4426950408889634f);
    float ls = e * lz;
    ls += dpp<0xB1>(ls); ls += dpp<0x4E>(ls); ls += dpp<0x141>(ls);
    const float wz = e / ls;
    u32x4 af[2];
#pragma unroll
    for (int kk = 0; kk < 2; ++kk)
#pragma unroll
      for (int u = 0; u < 4; ++u) af[kk][u] = pack2(gval(ga[kk * 4 + u].x) * wz, gval(ga[kk * 4 + u].z) * wz);
    float y[5];
#pragma unroll
    for (int t = 0; t < 5; ++t) {
      f32x4 d1 = {0.f, 0.f, 0.f, 0.f}, d2 = {0.f, 0.f, 0.f, 0.f};
      d1 = mfma(af[0], bf[t][0], d1); d2 = mfma(af[0], bf[t][2], d2);
      d1 = mfma(af[1], bf[t][1], d1); d2 = mfma(af[1], bf[t][3], d2);
      const float u1 = (d1[0] + d1[1]) + (d1[2] + d1[3]), u2 = (d2[0] + d2[1]) + (d2[2] + d2[3]);
      y[t] = xrow_sum(lane < 32 ? u1 : u2) + yb[t];
    }
    const float ysel = g4 == 0 ? y[0] : g4 == 1 ? y[1] : g4 == 2 ? y[2] : y[3];
    const u32 thi = tag.hi;
    u32x2 gr; gr.x = ((asu(ysel) + 0x80u) >> 8) | tag.lo; gr.y = thi;
    __builtin_amdgcn_raw_buffer_store_b64(gr, rws, WS_X3 + (((s * 5 + g4) * 32 + h) * 16 + (lane & 15)) * 8, 0, AUX_SC1);
    if (lane < 16) {
      gr.x = ((asu(y[4]) + 0x80u) >> 8) | tag.lo;
      __builtin_amdgcn_raw_buffer_store_b64(gr, rws, WS_X3 + (((s * 5 + 4) * 32 + h) * 16 + lane) * 8, 0, AUX_SC1);
    }
  }
  STAMP(8);
  if (h >= 5) return;
  {
    const int x3off = WS_X3 + (((s * 5 + h) * 32 + (lane >> 3)) * 16 + 2 * (lane & 7)) * 8;
    u32x4 gy[4];
    for (u32 spin = 0; spin < SPIN_CAP; ++spin) {
      u32 bad = 0;
      asm volatile("" ::: "memory");
#pragma unroll
      for (int i = 0; i < 4; ++i) gy[i] = __builtin_amdgcn_raw_buffer_load_b128(rws, x3off + i * 1024, 0, AUX_SC1);
#pragma unroll
      for (int i = 0; i < 4; ++i) bad |= gbad(gy[i], tag);
      if (__builtin_amdgcn_ballot_w64(bad != 0) == 0) break;
    }
    float y0 = (gval(gy[0].x) + gval(gy[1].x)) + (gval(gy[2].x) + gval(gy[3].x));
    float y1 = (gval(gy[0].z) + gval(gy[1].z)) + (gval(gy[2].z) + gval(gy[3].z));
    y0 += dpp<ROR8>(y0); y1 += dpp<ROR8>(y1);
    y0 = xrow_sum(y0); y1 = xrow_sum(y1);
    if (lane < 8) ((GAS u32*)a.out)[40 * s + 8 * h + lane] = pack2(lo16(xo) + y0, hi16(xo) + y1);
    if (b == 0 && lane == 0) {
      const u64 full = ((((u64)ep.y << 32) | ep.x) + 1);
      __hip_atomic_store((GAS u64*)(a.ws + WS_EPOCH), full, __ATOMIC_RELAXED, __HIP_MEMORY_SCOPE_AGENT);
    }
  }
  STAMP(9);
}

}

extern "C" void kernel_launch(void* const* d_in, const int* in_sizes, int n_in, void* d_out, int out_size, void* d_ws, size_t ws_size, hipStream_t stream) {
  fused::Args a;
  a.x = (const unsigned short*)d_in[0];
  a.K = (const unsigned short*)d_in[1];
  a.V = (const unsigned short*)d_in[2];
  a.wq = (const unsigned short*)d_in[3];
  a.wo = (const unsigned short*)d_in[4];
  a.w1 = (const unsigned short*)d_in[6];
  a.w3 = (const unsigned short*)d_in[7];
  a.w2 = (const unsigned short*)d_in[8];
  a.out = (unsigned short*)d_out;
  a.ws = (unsigned char*)d_ws;
  fused::Args d;
  unsigned char* w = (unsigned char*)d_ws;
  d.x = (const unsigned short*)(w + (228u << 20)); d.K = (const unsigned short*)(w + (64u << 20)); d.V = (const unsigned short*)(w + (128u << 20));
  d.wq = (const unsigned short*)(w + (200u << 20)); d.wo = (const unsigned short*)(w + (208u << 20)); d.w1 = (const unsigned short*)(w + (216u << 20));
  d.w3 = (const unsigned short*)(w + (220u << 20)); d.w2 = (const unsigned short*)(w + (224u << 20)); d.out = (unsigned short*)(w + (229u << 20)); d.ws = w + (232u << 20);
  hipLaunchKernelGGL(fused::fused_kernel, dim3(fused::NWG), dim3(fused::NT), fused::L_END, stream, d);
  hipLaunchKernelGGL(fused::fused_kernel, dim3(fused::NWG), dim3(fused::NT), fused::L_END, stream, a);
}
```

```cpp
#include <hip/hip_runtime.h>
#include <stdint.h>

namespace fused {

typedef unsigned u32;
typedef unsigned long long u64;
typedef u32 u32x2 __attribute__((ext_vector_type(2)));
typedef u32 u32x4 __attribute__((ext_vector_type(4)));
typedef float f32x2 __attribute__((ext_vector_type(2)));
typedef float f32x4 __attribute__((ext_vector_type(4)));
typedef short s16x4 __attribute__((ext_vector_type(4)));
typedef short s16x8 __attribute__((ext_vector_type(8)));
typedef __bf16 bf16x2 __attribute__((ext_vector_type(2)));
typedef __bf16 bf16x8 __attribute__((ext_vector_type(8)));
#define LAS __attribute__((address_space(3)))
#define GAS __attribute__((address_space(1)))

constexpr int D = 640, H = 32, S = 8192, DH = 128, F = 1728, HD = H * DH;
constexpr int NWG = 256, NT = 512;
constexpr int NG = F / 64;
constexpr int NP = 32;
#define G0 4
#define GP 4
#define GPOLL0 2
constexpr int NGRP = (NP - G0 + GP - 1) / GP;
constexpr u32 SPIN_CAP = 1u << 18;
constexpr int AUX_NT = 2, AUX_SC1 = 16, RSRC_W3 = 0x00020000;

constexpr int WS_EPOCH = 0;
constexpr int WS_X1 = 4096;
constexpr int WS_X2 = WS_X1 + NWG * 256 * 8;
constexpr int WS_ML = WS_X2 + NWG * 128 * 8;
constexpr int WS_X3 = WS_ML + NWG * 128;
constexpr int WS_END = WS_X3 + 8 * 5 * 32 * 16 * 8;
constexpr int WS_STAMP = 2 << 20;

constexpr int L_WO = 0;
constexpr int L_W2 = 128 * 160;
constexpr int L_PART = L_W2 + 64 * 160;
constexpr int L_OP = L_PART + 8192;
constexpr int L_Q = L_OP + 4096;
constexpr int L_HID = L_Q + 256;
constexpr int L_MW = L_HID + 128;
constexpr int L_LW = L_MW + 32;
constexpr int L_FLAG = L_LW + 32;
constexpr int L_END = L_FLAG + 16;

struct Args {
  const unsigned short *x, *K, *V, *wq, *wo, *w1, *w3, *w2;
  unsigned short* out;
  unsigned char* ws;
};

__device__ __forceinline__ float asf(u32 u) { return __builtin_bit_cast(float, u); }
__device__ __forceinline__ u32 asu(float f) { return __builtin_bit_cast(u32, f); }
__device__ __forceinline__ float lo16(u32 u) { return asf(u << 16); }
__device__ __forceinline__ float hi16(u32 u) { return asf(u & 0xffff0000u); }
__device__ __forceinline__ u32 pack2(float a, float b) { f32x2 v = {a, b}; return __builtin_bit_cast(u32, __builtin_convertvector(v, bf16x2)); }
__device__ __forceinline__ float rbf(float a) { return (float)(__bf16)a; }
__device__ __forceinline__ float dot2(u32 a, u32 b, float c) {
  return __builtin_amdgcn_fdot2_f32_bf16(__builtin_bit_cast(bf16x2, a), __builtin_bit_cast(bf16x2, b), c, false);
}
template <int CTRL> __device__ __forceinline__ float dpp(float x) {
  return asf((u32)__builtin_amdgcn_mov_dpp((int)asu(x), CTRL, 0xf, 0xf, true));
}
constexpr int ROR1 = 0x121, ROR2 = 0x122, ROR4 = 0x124, ROR8 = 0x128;
__device__ __forceinline__ float row_sum(float x) { x += dpp<ROR8>(x); x += dpp<ROR4>(x); x += dpp<ROR2>(x); x += dpp<ROR1>(x); return x; }
__device__ __forceinline__ float swap16_sum(float x) { auto s = __builtin_amdgcn_permlane16_swap(asu(x), asu(x), false, false); return asf(s[0]) + asf(s[1]); }
__device__ __forceinline__ float swap32_sum(float x) { auto s = __builtin_amdgcn_permlane32_swap(asu(x), asu(x), false, false); return asf(s[0]) + asf(s[1]); }
__device__ __forceinline__ float xrow_sum(float x) { return swap32_sum(swap16_sum(x)); }
__device__ __forceinline__ float xrow_max(float x) {
  auto s = __builtin_amdgcn_permlane16_swap(asu(x), asu(x), false, false); x = fmaxf(asf(s[0]), asf(s[1]));
  auto t = __builtin_amdgcn_permlane32_swap(asu(x), asu(x), false, false); return fmaxf(asf(t[0]), asf(t[1]));
}
__device__ __forceinline__ float wave_sum(float x) { return xrow_sum(row_sum(x)); }
__device__ __forceinline__ f32x4 mfma(u32x4 a, u32x4 b, f32x4 c) {
  return __builtin_amdgcn_mfma_f32_16x16x32_bf16(__builtin_bit_cast(bf16x8, a), __builtin_bit_cast(bf16x8, b), c, 0, 0, 0);
}
__device__ __forceinline__ void lds_barrier() {
  asm volatile("s_waitcnt lgkmcnt(0)" ::: "memory");
  __builtin_amdgcn_s_barrier();
  asm volatile("" ::: "memory");
}
#define FENCE() do { asm volatile("" ::: "memory"); __builtin_amdgcn_sched_barrier(0); } while (0)

struct Tag { u32 lo, hi; };
__device__ __forceinline__ u32x4 gpack(float a, float b, Tag t) {
  u32x4 r; r.x = ((asu(a) + 0x80u) >> 8) | t.lo; r.y = t.hi; r.z = ((asu(b) + 0x80u) >> 8) | t.lo; r.w = t.hi; return r;
}
__device__ __forceinline__ u32 gbad(u32x4 g, Tag t) { return (((g.x ^ t.lo) | (g.z ^ t.lo)) >> 24) | (g.y ^ t.hi) | (g.w ^ t.hi); }
__device__ __forceinline__ float gval(u32 w) { return asf(w << 8); }

#define STAMP(i) do { } while (0)

__device__ __forceinline__ u32x4 bfrag(LAS unsigned char* img, int lane, int k0, int n0) {
  const int g = lane >> 4, q = (lane >> 2) & 3, p = lane & 3;
  LAS unsigned char* ad = img + (k0 + 8 * g + q) * 160 + (n0 + 4 * p) * 2;
  s16x4 t0 = __builtin_amdgcn_ds_read_tr16_b64_v4i16((LAS s16x4*)ad);
  s16x4 t1 = __builtin_amdgcn_ds_read_tr16_b64_v4i16((LAS s16x4*)(ad + 4 * 160));
  s16x8 r = __builtin_shufflevector(t0, t1, 0, 1, 2, 3, 4, 5, 6, 7);
  return __builtin_bit_cast(u32x4, r);
}

__global__ void __launch_bounds__(NT, 2) fused_kernel(Args a) {
  extern __shared__ __attribute__((aligned(16))) unsigned char smem[];
  LAS unsigned char* L = (LAS unsigned char*)smem;
  const int tid = threadIdx.x, lane = tid & 63, wave = __builtin_amdgcn_readfirstlane(tid >> 6);
  const int b = blockIdx.x, h = b >> 3, s = b & 7;
  const bool mlp = h < NG;
  asm volatile("" :: "s"(a.x), "s"(a.K), "s"(a.V), "s"(a.wq), "s"(a.wo), "s"(a.w1), "s"(a.w3), "s"(a.w2), "s"(a.out), "s"(a.ws));
  STAMP(0);

  const __amdgpu_buffer_rsrc_t rws = __builtin_amdgcn_make_buffer_rsrc((void*)a.ws, 0, WS_END, RSRC_W3);
  const __amdgpu_buffer_rsrc_t rk = __builtin_amdgcn_make_buffer_rsrc((void*)a.K, 0, H * S * DH * 2, RSRC_W3);
  const __amdgpu_buffer_rsrc_t rv = __builtin_amdgcn_make_buffer_rsrc((void*)a.V, 0, H * S * DH * 2, RSRC_W3);

  const u32x2 ep = __builtin_amdgcn_raw_buffer_load_b64(rws, WS_EPOCH, 0, AUX_SC1);
  u32 xs[5], xr[5];
#pragma unroll
  for (int i = 0; i < 5; ++i) xs[i] = ((const GAS u32*)a.x)[lane + 64 * i];
  const int prow = 80 * s + (tid >> 5), c32 = tid & 31;
#pragma unroll
  for (int i = 0; i < 5; ++i) xr[i] = ((const GAS unsigned short*)a.x)[prow + 16 * i];
  u32x4 wv[5];
  {
    const bool isq = c32 < 16 || !mlp;
    const GAS unsigned short* base = isq ? (const GAS unsigned short*)a.wq + h * DH + 8 * (c32 & 15)
                                         : (c32 < 24 ? (const GAS unsigned short*)a.w1 + 64 * h + 8 * (c32 - 16) : (const GAS unsigned short*)a.w3 + 64 * h + 8 * (c32 - 24));
    const int stride = isq ? HD : F;
    base += (size_t)prow * stride;
#pragma unroll
    for (int i = 0; i < 5; ++i) wv[i] = __builtin_nontemporal_load((const GAS u32x4*)(base + 16 * i * stride));
  }
  u32x4 wt[4];
#pragma unroll
  for (int j = 0; j < 4; ++j) {
    const int it = tid + 512 * j;
    const bool isw2 = it >= 1280 && it < 1920 && mlp;
    const int i2 = it < 1280 ? it : it - 1280;
    const int row = i2 / 10, ch = i2 - row * 10;
    const GAS unsigned short* p = isw2 ? (const GAS unsigned short*)a.w2 + (size_t)(64 * h + row) * D : (const GAS unsigned short*)a.wo + (size_t)(DH * h + row) * D;
    wt[j] = __builtin_nontemporal_load((const GAS u32x4*)(p + 80 * s + 8 * ch));
  }
  const u32 xo = ((const GAS u32*)a.x)[40 * s + 8 * (h < 5 ? h : 0) + (lane & 7)];
  FENCE();

  const int piece0 = h * 2048 + s * 256 + wave * NP;
  const int j0 = (h + 3 * s + 5 * wave) & (NP - 1);
  const int voff = lane * 16;
  u32x4 kv[NP];
#define POFF(i) ((piece0 + ((j0 + (i)) & (NP - 1))) << 10)
#pragma unroll
  for (int i = 0; i < G0; ++i) kv[i] = __builtin_amdgcn_raw_buffer_load_b128(rk, voff, POFF(i), AUX_NT);
  FENCE();

  float xn[5];
  {
    float sum = 0.f;
#pragma unroll
    for (int i = 0; i < 5; ++i) sum += lo16(xs[i]) + hi16(xs[i]);
    const float mean = wave_sum(sum) * (1.f / D);
    float sq = 0.f;
#pragma unroll
    for (int i = 0; i < 5; ++i) { const float d0 = lo16(xs[i]) - mean, d1 = hi16(xs[i]) - mean; sq += d0 * d0 + d1 * d1; }
    const float var = wave_sum(sq) * (1.f / D);
    const float mb = rbf(mean), sd = rbf(sqrtf(rbf(rbf(var) + 1e-5f)));
#pragma unroll
    for (int i = 0; i < 5; ++i) xn[i] = rbf(rbf(lo16(xr[i]) - mb) / sd);
  }
  {
    float acc[8];
#pragma unroll
    for (int j = 0; j < 8; ++j) acc[j] = 0.f;
#pragma unroll
    for (int i = 0; i < 5; ++i) {
      const u32 ph = asu(xn[i]), pl = ph >> 16;
#pragma unroll
      for (int j = 0; j < 4; ++j) { acc[2 * j] = dot2(wv[i][j], pl, acc[2 * j]); acc[2 * j + 1] = dot2(wv[i][j], ph, acc[2 * j + 1]); }
    }
#pragma unroll
    for (int j = 0; j < 8; ++j) acc[j] = swap32_sum(acc[j]);
    const bool up = lane >= 32;
    f32x4 o4 = {up ? acc[4] : acc[0], up ? acc[5] : acc[1], up ? acc[6] : acc[2], up ? acc[7] : acc[3]};
    *(LAS f32x4*)(L + L_PART + (wave * 256 + 8 * (lane & 31) + (up ? 4 : 0)) * 4) = o4;
    if (tid == 0) *(volatile LAS u32*)(L + L_FLAG) = 0u;
  }
  STAMP(1);
  lds_barrier();
  Tag tag;
  {
    const u32 e0 = __builtin_amdgcn_readfirstlane(ep.x), e1 = __builtin_amdgcn_readfirstlane(ep.y);
    const u64 e = (((u64)e1 << 32) | e0) + 1;
    tag.lo = (u32)(e & 0xff) << 24; tag.hi = (u32)(e >> 8);
  }
  if (wave == 0 || (wave == 1 && mlp)) {
    f32x2 sum = {0.f, 0.f};
#pragma unroll
    for (int w = 0; w < 8; ++w) sum += *(LAS f32x2*)(L + L_PART + (w * 256 + 2 * tid) * 4);
    __builtin_amdgcn_raw_buffer_store_b128(gpack(sum.x, sum.y, tag), rws, WS_X1 + (b * 256 + 2 * tid) * 8, 0, AUX_SC1);
  }
  STAMP(2);
  FENCE();

  bool mydone = !(wave == 0 || (wave == 1 && mlp));
  bool qready = false;
  const int x1off = WS_X1 + (h * 8 * 256 + wave * 128 + 2 * lane) * 8;
  auto finish_gather = [&](const u32x4 (&pq)[8]) {
    float s0 = 0.f, s1 = 0.f;
#pragma unroll
    for (int z = 0; z < 8; ++z) { s0 += gval(pq[z].x); s1 += gval(pq[z].z); }
    if (wave == 0) {
      *(volatile LAS u32*)(L + L_Q + lane * 4) = pack2(s0, s1);
      asm volatile("s_waitcnt lgkmcnt(0)" ::: "memory");
      *(volatile LAS u32*)(L + L_FLAG) = 1u;
    } else {
      const float g0 = rbf(s0), g1 = rbf(s1);
      auto t0 = __builtin_amdgcn_permlane32_swap(asu(g0), asu(g0), false, false);
      auto t1 = __builtin_amdgcn_permlane32_swap(asu(g1), asu(g1), false, false);
      const float ga = asf(t0[0]), ua = asf(t0[1]), gb = asf(t1[0]), ub = asf(t1[1]);
      const float ha = rbf(ga / (1.f + __expf(-ga))) * ua, hb = rbf(gb / (1.f + __expf(-gb))) * ub;
      if (lane < 32) *(volatile LAS u32*)(L + L_HID + lane * 4) = pack2(ha, hb);
    }
  };
#pragma unroll
  for (int g = 1; g <= NGRP; ++g) {
    const bool poll = !mydone && g >= GPOLL0;
    u32x4 pq[8];
    if (poll) {
#pragma unroll
      for (int z = 0; z < 8; ++z) pq[z] = __builtin_amdgcn_raw_buffer_load_b128(rws, x1off + z * 2048, 0, AUX_SC1);
    }
    FENCE();
#pragma unroll
    for (int i = G0 + (g - 1) * GP; i < G0 + g * GP && i < NP; ++i) kv[i] = __builtin_amdgcn_raw_buffer_load_b128(rk, voff, POFF(i), AUX_NT);
    FENCE();
    if (g == 1) {
#pragma unroll
      for (int j = 0; j < 4; ++j) if (tid + 512 * j < 1920) *(LAS u32x4*)(L + L_WO + (tid + 512 * j) * 16) = wt[j];
    }
    if (poll) {
      u32 bad = 0;
#pragma unroll
      for (int z = 0; z < 8; ++z) bad |= gbad(pq[z], tag);
      if (__builtin_amdgcn_ballot_w64(bad != 0) == 0) { finish_gather(pq); mydone = true; STAMP(10); }
    } else if (!qready) {
      asm volatile("s_waitcnt vmcnt(%0)" :: "n"(GP) : "memory");
    }
    if (!qready) qready = __builtin_amdgcn_readfirstlane(*(volatile LAS u32*)(L + L_FLAG)) != 0;
    FENCE();
  }
  if (!mydone) {
    for (u32 spin = 0; spin < SPIN_CAP; ++spin) {
      u32x4 pq[8];
      asm volatile("" ::: "memory");
#pragma unroll
      for (int z = 0; z < 8; ++z) pq[z] = __builtin_amdgcn_raw_buffer_load_b128(rws, x1off + z * 2048, 0, AUX_SC1);
      u32 bad = 0;
#pragma unroll
      for (int z = 0; z < 8; ++z) bad |= gbad(pq[z], tag);
      if (__builtin_amdgcn_ballot_w64(bad != 0) == 0) { finish_gather(pq); break; }
    }
  }
  if (!qready) {
    for (u32 spin = 0; spin < (SPIN_CAP << 4); ++spin) {
      if (__builtin_amdgcn_readfirstlane(*(volatile LAS u32*)(L + L_FLAG)) != 0) break;
      __builtin_amdgcn_s_sleep(1);
    }
  }
  asm volatile("" ::: "memory");
  const u32x4 qv = *(LAS u32x4*)(L + L_Q + (lane & 15) * 16);
  STAMP(3);
  FENCE();

  float sc[NP];
#pragma unroll
  for (int i = 0; i < NP; ++i) {
    float d = dot2(kv[i].x, qv.x, 0.f);
    d = dot2(kv[i].y, qv.y, d); d = dot2(kv[i].z, qv.z, d); d = dot2(kv[i].w, qv.w, d);
    kv[i] = __builtin_amdgcn_raw_buffer_load_b128(rv, voff, POFF(i), AUX_NT);
    d = row_sum(d);
    sc[i] = rbf(rbf(d) * 0.08838834764831845f);
    if ((i & 1) == 1) FENCE();
  }
  float mw = sc[0];
#pragma unroll
  for (int i = 1; i < NP; ++i) mw = fmaxf(mw, sc[i]);
  mw = xrow_max(mw);
  if (lane == 0) *(LAS float*)(L + L_MW + wave * 4) = mw;
  STAMP(4);
  lds_barrier();
  float M;
  {
    const f32x4 m0 = *(LAS f32x4*)(L + L_MW), m1 = *(LAS f32x4*)(L + L_MW + 16);
    M = fmaxf(fmaxf(fmaxf(m0.x, m0.y), fmaxf(m0.z, m0.w)), fmaxf(fmaxf(m1.x, m1.y), fmaxf(m1.z, m1.w)));
  }
  float lsum = 0.f;
#pragma unroll
  for (int i = 0; i < NP; ++i) {
    const float p = __builtin_amdgcn_exp2f((sc[i] - M) * 1.4426950408889634f);
    lsum += p;
    sc[i] = rbf(p);
  }
  lsum = xrow_sum(lsum);
  if (lane == 0) *(LAS float*)(L + L_LW + wave * 4) = lsum;
  FENCE();

  float acc[8];
#pragma unroll
  for (int j = 0; j < 8; ++j) acc[j] = 0.f;
#pragma unroll
  for (int i = 0; i < NP; ++i) {
    const u32 ph = asu(sc[i]), pl = ph >> 16;
#pragma unroll
    for (int j = 0; j < 4; ++j) { acc[2 * j] = dot2(kv[i][j], pl, acc[2 * j]); acc[2 * j + 1] = dot2(kv[i][j], ph, acc[2 * j + 1]); }
    if ((i & 1) == 1) FENCE();
  }
#pragma unroll
  for (int j = 0; j < 8; ++j) acc[j] = xrow_sum(acc[j]);
  if (lane < 32) {
    const bool up = lane >= 16;
    f32x4 o4 = {up ? acc[4] : acc[0], up ? acc[5] : acc[1], up ? acc[6] : acc[2], up ? acc[7] : acc[3]};
    *(LAS f32x4*)(L + L_OP + (wave * 128 + 8 * (lane & 15) + (up ? 4 : 0)) * 4) = o4;
  }
  STAMP(5);
  lds_barrier();
  if (wave != 0) return;

  {
    f32x2 o2 = {0.f, 0.f};
#pragma unroll
    for (int w = 0; w < 8; ++w) o2 += *(LAS f32x2*)(L + L_OP + (w * 128 + 2 * lane) * 4);
    __builtin_amdgcn_raw_buffer_store_b128(gpack(o2.x, o2.y, tag), rws, WS_X2 + (b * 128 + 2 * lane) * 8, 0, AUX_SC1);
    const f32x4 l0 = *(LAS f32x4*)(L + L_LW), l1 = *(LAS f32x4*)(L + L_LW + 16);
    const float lw = ((l0.x + l0.y) + (l0.z + l0.w)) + ((l1.x + l1.y) + (l1.z + l1.w));
    if (lane == 0) __builtin_amdgcn_raw_buffer_store_b128(gpack(M, lw, tag), rws, WS_ML + b * 128, 0, AUX_SC1);
  }
  STAMP(6);
  const int pz = lane & 7, half = (lane >> 3) & 1, g4 = lane >> 4;
  const int x2off = WS_X2 + ((h * 8 + pz) * 128 + 64 * half + 8 * g4) * 8;
  const int mloff = WS_ML + (h * 8 + pz) * 128;
  u32x4 ga[8], gm;
#define HOP1_LOADS() do { \
    _Pragma("unroll") for (int kk = 0; kk < 2; ++kk) \
      _Pragma("unroll") for (int u = 0; u < 4; ++u) ga[kk * 4 + u] = __builtin_amdgcn_raw_buffer_load_b128(rws, x2off + kk * 256 + u * 16, 0, AUX_SC1); \
    gm = __builtin_amdgcn_raw_buffer_load_b128(rws, mloff, 0, AUX_SC1); } while (0)
  HOP1_LOADS();
  FENCE();
  float yb[5];
  {
    if (mlp) {
      const u32x4 ha0 = *(LAS u32x4*)(L + L_HID + g4 * 16), ha1 = *(LAS u32x4*)(L + L_HID + 64 + g4 * 16);
#pragma unroll
      for (int t = 0; t < 5; ++t) {
        f32x4 d = {0.f, 0.f, 0.f, 0.f};
        d = mfma(ha0, bfrag(L + L_W2, lane, 0, 16 * t), d);
        d = mfma(ha1, bfrag(L + L_W2, lane, 32, 16 * t), d);
        yb[t] = d[0];
      }
    } else {
#pragma unroll
      for (int t = 0; t < 5; ++t) yb[t] = 0.f;
    }
  }
  u32x4 bf[5][4];
#pragma unroll
  for (int t = 0; t < 5; ++t)
#pragma unroll
    for (int ks = 0; ks < 4; ++ks) bf[t][ks] = bfrag(L + L_WO, lane, 32 * ks, 16 * t);
  FENCE();
  for (u32 spin = 0; spin < SPIN_CAP; ++spin) {
    u32 bad = gbad(gm, tag);
#pragma unroll
    for (int u = 0; u < 8; ++u) bad |= gbad(ga[u], tag);
    if (__builtin_amdgcn_ballot_w64(bad != 0) == 0) break;
    asm volatile("" ::: "memory");
    HOP1_LOADS();
  }
  STAMP(7);
  {
    const float mz = gval(gm.x), lz = gval(gm.z);
    float mx = mz;
    mx = fmaxf(mx, dpp<0xB1>(mx)); mx = fmaxf(mx, dpp<0x4E>(mx)); mx = fmaxf(mx, dpp<0x141>(mx));
    const float e = __builtin_amdgcn_exp2f((mz - mx) * 1.4426950408889634f);
    float ls = e * lz;
    ls += dpp<0xB1>(ls); ls += dpp<0x4E>(ls); ls += dpp<0x141>(ls);
    const float wz = e / ls;
    u32x4 af[2];
#pragma unroll
    for (int kk = 0; kk < 2; ++kk)
#pragma unroll
      for (int u = 0; u < 4; ++u) af[kk][u] = pack2(gval(ga[kk * 4 + u].x) * wz, gval(ga[kk * 4 + u].z) * wz);
    float y[5];
#pragma unroll
    for (int t = 0; t < 5; ++t) {
      f32x4 d1 = {0.f, 0.f, 0.f, 0.f}, d2 = {0.f, 0.f, 0.f, 0.f};
      d1 = mfma(af[0], bf[t][0], d1); d2 = mfma(af[0], bf[t][2], d2);
      d1 = mfma(af[1], bf[t][1], d1); d2 = mfma(af[1], bf[t][3], d2);
      const float u1 = (d1[0] + d1[1]) + (d1[2] + d1[3]), u2 = (d2[0] + d2[1]) + (d2[2] + d2[3]);
      y[t] = xrow_sum(lane < 32 ? u1 : u2) + yb[t];
    }
    const float ysel = g4 == 0 ? y[0] : g4 == 1 ? y[1] : g4 == 2 ? y[2] : y[3];
    const u32 thi = tag.hi;
    u32x2 gr; gr.x = ((asu(ysel) + 0x80u) >> 8) | tag.lo; gr.y = thi;
    __builtin_amdgcn_raw_buffer_store_b64(gr, rws, WS_X3 + (((s * 5 + g4) * 32 + h) * 16 + (lane & 15)) * 8, 0, AUX_SC1);
    if (lane < 16) {
      gr.x = ((asu(y[4]) + 0x80u) >> 8) | tag.lo;
      __builtin_amdgcn_raw_buffer_store_b64(gr, rws, WS_X3 + (((s * 5 + 4) * 32 + h) * 16 + lane) * 8, 0, AUX_SC1);
    }
  }
  STAMP(8);
  if (h >= 5) return;
  {
    const int x3off = WS_X3 + (((s * 5 + h) * 32 + (lane >> 3)) * 16 + 2 * (lane & 7)) * 8;
    u32x4 gy[4];
    for (u32 spin = 0; spin < SPIN_CAP; ++spin) {
      u32 bad = 0;
      asm volatile("" ::: "memory");
#pragma unroll
      for (int i = 0; i < 4; ++i) gy[i] = __builtin_amdgcn_raw_buffer_load_b128(rws, x3off + i * 1024, 0, AUX_SC1);
#pragma unroll
      for (int i = 0; i < 4; ++i) bad |= gbad(gy[i], tag);
      if (__builtin_amdgcn_ballot_w64(bad != 0) == 0) break;
    }
    float y0 = (gval(gy[0].x) + gval(gy[1].x)) + (gval(gy[2].x) + gval(gy[3].x));
    float y1 = (gval(gy[0].z) + gval(gy[1].z)) + (gval(gy[2].z) + gval(gy[3].z));
    y0 += dpp<ROR8>(y0); y1 += dpp<ROR8>(y1);
    y0 = xrow_sum(y0); y1 = xrow_sum(y1);
    if (lane < 8) ((GAS u32*)a.out)[40 * s + 8 * h + lane] = pack2(lo16(xo) + y0, hi16(xo) + y1);
    if (b == 0 && lane == 0) {
      const u64 full = ((((u64)ep.y << 32) | ep.x) + 1);
      __hip_atomic_store((GAS u64*)(a.ws + WS_EPOCH), full, __ATOMIC_RELAXED, __HIP_MEMORY_SCOPE_AGENT);
    }
  }
  STAMP(9);
}

template <int NTL> __global__ void preread_kernel(const u32x4* p, unsigned nvec, unsigned ticks, unsigned* sink) {
  const unsigned long long t0 = __builtin_amdgcn_s_memrealtime();
  u32x4 acc = {0, 0, 0, 0};
  for (unsigned i = blockIdx.x * blockDim.x + threadIdx.x; i < nvec; i += gridDim.x * blockDim.x) {
    if (NTL) acc ^= __builtin_nontemporal_load((const GAS u32x4*)p + i); else acc ^= ((const GAS u32x4*)p)[i];
  }
  if ((acc.x ^ acc.y ^ acc.z ^ acc.w) == 0x9e3779b9u) *sink = acc.x;
  while (__builtin_amdgcn_s_memrealtime() - t0 < ticks) __builtin_amdgcn_s_sleep(1);
}
}

extern "C" void kernel_launch(void* const* d_in, const int* in_sizes, int n_in, void* d_out, int out_size, void* d_ws, size_t ws_size, hipStream_t stream) {
  fused::Args a;
  a.x = (const unsigned short*)d_in[0];
  a.K = (const unsigned short*)d_in[1];
  a.V = (const unsigned short*)d_in[2];
  a.wq = (const unsigned short*)d_in[3];
  a.wo = (const unsigned short*)d_in[4];
  a.w1 = (const unsigned short*)d_in[6];
  a.w3 = (const unsigned short*)d_in[7];
  a.w2 = (const unsigned short*)d_in[8];
  a.out = (unsigned short*)d_out;
  a.ws = (unsigned char*)d_ws;
  hipLaunchKernelGGL(fused::preread_kernel<1>, dim3(256), dim3(1024), 0, stream, (const fused::u32x4*)((char*)d_ws + (64u << 20)), 524288u, 10000u, (unsigned*)((char*)d_ws + (8 << 20)));
  hipLaunchKernelGGL(fused::fused_kernel, dim3(fused::NWG), dim3(fused::NT), fused::L_END, stream, a);
}
```

```cpp
#define PADT 6000
#include <hip/hip_runtime.h>
#include <stdint.h>

namespace fused {

typedef unsigned u32;
typedef unsigned long long u64;
typedef u32 u32x2 __attribute__((ext_vector_type(2)));
typedef u32 u32x4 __attribute__((ext_vector_type(4)));
typedef float f32x2 __attribute__((ext_vector_type(2)));
typedef float f32x4 __attribute__((ext_vector_type(4)));
typedef short s16x4 __attribute__((ext_vector_type(4)));
typedef short s16x8 __attribute__((ext_vector_type(8)));
typedef __bf16 bf16x2 __attribute__((ext_vector_type(2)));
typedef __bf16 bf16x8 __attribute__((ext_vector_type(8)));
#define LAS __attribute__((address_space(3)))
#define GAS __attribute__((address_space(1)))

constexpr int D = 640, H = 32, S = 8192, DH = 128, F = 1728, HD = H * DH;
constexpr int NWG = 256, NT = 512;
constexpr int NG = F / 64;
constexpr int NP = 32;
#define G0 4
#define GP 4
#define GPOLL0 2
constexpr int NGRP = (NP - G0 + GP - 1) / GP;
constexpr u32 SPIN_CAP = 1u << 18;
constexpr int AUX_NT = 2, AUX_SC1 = 16, RSRC_W3 = 0x00020000;

constexpr int WS_EPOCH = 0;
constexpr int WS_X1 = 4096;
constexpr int WS_X2 = WS_X1 + NWG * 256 * 8;
constexpr int WS_ML = WS_X2 + NWG * 128 * 8;
constexpr int WS_X3 = WS_ML + NWG * 128;
constexpr int WS_END = WS_X3 + 8 * 5 * 32 * 16 * 8;
constexpr int WS_STAMP = 2 << 20;

constexpr int L_WO = 0;
constexpr int L_W2 = 128 * 160;
constexpr int L_PART = L_W2 + 64 * 160;
constexpr int L_OP = L_PART + 8192;
constexpr int L_Q = L_OP + 4096;
constexpr int L_HID = L_Q + 256;
constexpr int L_MW = L_HID + 128;
constexpr int L_LW = L_MW + 32;
constexpr int L_FLAG = L_LW + 32;
constexpr int L_END = L_FLAG + 16;

struct Args {
  const unsigned short *x, *K, *V, *wq, *wo, *w1, *w3, *w2;
  unsigned short* out;
  unsigned char* ws;
};

__device__ __forceinline__ float asf(u32 u) { return __builtin_bit_cast(float, u); }
__device__ __forceinline__ u32 asu(float f) { return __builtin_bit_cast(u32, f); }
__device__ __forceinline__ float lo16(u32 u) { return asf(u << 16); }
__device__ __forceinline__ float hi16(u32 u) { return asf(u & 0xffff0000u); }
__device__ __forceinline__ u32 pack2(float a, float b) { f32x2 v = {a, b}; return __builtin_bit_cast(u32, __builtin_convertvector(v, bf16x2)); }
__device__ __forceinline__ float rbf(float a) { return (float)(__bf16)a; }
__device__ __forceinline__ float dot2(u32 a, u32 b, float c) {
  return __builtin_amdgcn_fdot2_f32_bf16(__builtin_bit_cast(bf16x2, a), __builtin_bit_cast(bf16x2, b), c, false);
}
template <int CTRL> __device__ __forceinline__ float dpp(float x) {
  return asf((u32)__builtin_amdgcn_mov_dpp((int)asu(x), CTRL, 0xf, 0xf, true));
}
constexpr int ROR1 = 0x121, ROR2 = 0x122, ROR4 = 0x124, ROR8 = 0x128;
__device__ __forceinline__ float row_sum(float x) { x += dpp<ROR8>(x); x += dpp<ROR4>(x); x += dpp<ROR2>(x); x += dpp<ROR1>(x); return x; }
__device__ __forceinline__ float swap16_sum(float x) { auto s = __builtin_amdgcn_permlane16_swap(asu(x), asu(x), false, false); return asf(s[0]) + asf(s[1]); }
__device__ __forceinline__ float swap32_sum(float x) { auto s = __builtin_amdgcn_permlane32_swap(asu(x), asu(x), false, false); return asf(s[0]) + asf(s[1]); }
__device__ __forceinline__ float xrow_sum(float x) { return swap32_sum(swap16_sum(x)); }
__device__ __forceinline__ float xrow_max(float x) {
  auto s = __builtin_amdgcn_permlane16_swap(asu(x), asu(x), false, false); x = fmaxf(asf(s[0]), asf(s[1]));
  auto t = __builtin_amdgcn_permlane32_swap(asu(x), asu(x), false, false); return fmaxf(asf(t[0]), asf(t[1]));
}
__device__ __forceinline__ float wave_sum(float x) { return xrow_sum(row_sum(x)); }
__device__ __forceinline__ f32x4 mfma(u32x4 a, u32x4 b, f32x4 c) {
  return __builtin_amdgcn_mfma_f32_16x16x32_bf16(__builtin_bit_cast(bf16x8, a), __builtin_bit_cast(bf16x8, b), c, 0, 0, 0);
}
__device__ __forceinline__ void lds_barrier() {
  asm volatile("s_waitcnt lgkmcnt(0)" ::: "memory");
  __builtin_amdgcn_s_barrier();
  asm volatile("" ::: "memory");
}
#define FENCE() do { asm volatile("" ::: "memory"); __builtin_amdgcn_sched_barrier(0); } while (0)

struct Tag { u32 lo, hi; };
__device__ __forceinline__ u32x4 gpack(float a, float b, Tag t) {
  u32x4 r; r.x = ((asu(a) + 0x80u) >> 8) | t.lo; r.y = t.hi; r.z = ((asu(b) + 0x80u) >> 8) | t.lo; r.w = t.hi; return r;
}
__device__ __forceinline__ u32 gbad(u32x4 g, Tag t) { return (((g.x ^ t.lo) | (g.z ^ t.lo)) >> 24) | (g.y ^ t.hi) | (g.w ^ t.hi); }
__device__ __forceinline__ float gval(u32 w) { return asf(w << 8); }

#define STAMP(i) do { } while (0)

__device__ __forceinline__ u32x4 bfrag(LAS unsigned char* img, int lane, int k0, int n0) {
  const int g = lane >> 4, q = (lane >> 2) & 3, p = lane & 3;
  LAS unsigned char* ad = img + (k0 + 8 * g + q) * 160 + (n0 + 4 * p) * 2;
  s16x4 t0 = __builtin_amdgcn_ds_read_tr16_b64_v4i16((LAS s16x4*)ad);
  s16x4 t1 = __builtin_amdgcn_ds_read_tr16_b64_v4i16((LAS s16x4*)(ad + 4 * 160));
  s16x8 r = __builtin_shufflevector(t0, t1, 0, 1, 2, 3, 4, 5, 6, 7);
  return __builtin_bit_cast(u32x4, r);
}

__global__ void __launch_bounds__(NT, 2) fused_kernel(Args a) {
  extern __shared__ __attribute__((aligned(16))) unsigned char smem[];
  LAS unsigned char* L = (LAS unsigned char*)smem;
  const int tid = threadIdx.x, lane = tid & 63, wave = __builtin_amdgcn_readfirstlane(tid >> 6);
  const int b = blockIdx.x, h = b >> 3, s = b & 7;
  const bool mlp = h < NG;
  asm volatile("" :: "s"(a.x), "s"(a.K), "s"(a.V), "s"(a.wq), "s"(a.wo), "s"(a.w1), "s"(a.w3), "s"(a.w2), "s"(a.out), "s"(a.ws));
  const unsigned long long tstart_ = __builtin_amdgcn_s_memrealtime();
  STAMP(0);

  const __amdgpu_buffer_rsrc_t rws = __builtin_amdgcn_make_buffer_rsrc((void*)a.ws, 0, WS_END, RSRC_W3);
  const __amdgpu_buffer_rsrc_t rk = __builtin_amdgcn_make_buffer_rsrc((void*)a.K, 0, H * S * DH * 2, RSRC_W3);
  const __amdgpu_buffer_rsrc_t rv = __builtin_amdgcn_make_buffer_rsrc((void*)a.V, 0, H * S * DH * 2, RSRC_W3);

  const u32x2 ep = __builtin_amdgcn_raw_buffer_load_b64(rws, WS_EPOCH, 0, AUX_SC1);
  u32 xs[5], xr[5];
#pragma unroll
  for (int i = 0; i < 5; ++i) xs[i] = ((const GAS u32*)a.x)[lane + 64 * i];
  const int prow = 80 * s + (tid >> 5), c32 = tid & 31;
#pragma unroll
  for (int i = 0; i < 5; ++i) xr[i] = ((const GAS unsigned short*)a.x)[prow + 16 * i];
  u32x4 wv[5];
  {
    const bool isq = c32 < 16 || !mlp;
    const GAS unsigned short* base = isq ? (const GAS unsigned short*)a.wq + h * DH + 8 * (c32 & 15)
                                         : (c32 < 24 ? (const GAS unsigned short*)a.w1 + 64 * h + 8 * (c32 - 16) : (const GAS unsigned short*)a.w3 + 64 * h + 8 * (c32 - 24));
    const int stride = isq ? HD : F;
    base += (size_t)prow * stride;
#pragma unroll
    for (int i = 0; i < 5; ++i) wv[i] = __builtin_nontemporal_load((const GAS u32x4*)(base + 16 * i * stride));
  }
  u32x4 wt[4];
#pragma unroll
  for (int j = 0; j < 4; ++j) {
    const int it = tid + 512 * j;
    const bool isw2 = it >= 1280 && it < 1920 && mlp;
    const int i2 = it < 1280 ? it : it - 1280;
    const int row = i2 / 10, ch = i2 - row * 10;
    const GAS unsigned short* p = isw2 ? (const GAS unsigned short*)a.w2 + (size_t)(64 * h + row) * D : (const GAS unsigned short*)a.wo + (size_t)(DH * h + row) * D;
    wt[j] = __builtin_nontemporal_load((const GAS u32x4*)(p + 80 * s + 8 * ch));
  }
  const u32 xo = ((const GAS u32*)a.x)[40 * s + 8 * (h < 5 ? h : 0) + (lane & 7)];
  FENCE();

  const int piece0 = h * 2048 + s * 256 + wave * NP;
  const int j0 = (h + 3 * s + 5 * wave) & (NP - 1);
  const int voff = lane * 16;
  u32x4 kv[NP];
#define POFF(i) ((piece0 + ((j0 + (i)) & (NP - 1))) << 10)
#pragma unroll
  for (int i = 0; i < G0; ++i) kv[i] = __builtin_amdgcn_raw_buffer_load_b128(rk, voff, POFF(i), AUX_NT);
  FENCE();

  float xn[5];
  {
    float sum = 0.f;
#pragma unroll
    for (int i = 0; i < 5; ++i) sum += lo16(xs[i]) + hi16(xs[i]);
    const float mean = wave_sum(sum) * (1.f / D);
    float sq = 0.f;
#pragma unroll
    for (int i = 0; i < 5; ++i) { const float d0 = lo16(xs[i]) - mean, d1 = hi16(xs[i]) - mean; sq += d0 * d0 + d1 * d1; }
    const float var = wave_sum(sq) * (1.f / D);
    const float mb = rbf(mean), sd = rbf(sqrtf(rbf(rbf(var) + 1e-5f)));
#pragma unroll
    for (int i = 0; i < 5; ++i) xn[i] = rbf(rbf(lo16(xr[i]) - mb) / sd);
  }
  {
    float acc[8];
#pragma unroll
    for (int j = 0; j < 8; ++j) acc[j] = 0.f;
#pragma unroll
    for (int i = 0; i < 5; ++i) {
      const u32 ph = asu(xn[i]), pl = ph >> 16;
#pragma unroll
      for (int j = 0; j < 4; ++j) { acc[2 * j] = dot2(wv[i][j], pl, acc[2 * j]); acc[2 * j + 1] = dot2(wv[i][j], ph, acc[2 * j + 1]); }
    }
#pragma unroll
    for (int j = 0; j < 8; ++j) acc[j] = swap32_sum(acc[j]);
    const bool up = lane >= 32;
    f32x4 o4 = {up ? acc[4] : acc[0], up ? acc[5] : acc[1], up ? acc[6] : acc[2], up ? acc[7] : acc[3]};
    *(LAS f32x4*)(L + L_PART + (wave * 256 + 8 * (lane & 31) + (up ? 4 : 0)) * 4) = o4;
    if (tid == 0) *(volatile LAS u32*)(L + L_FLAG) = 0u;
  }
  STAMP(1);
  lds_barrier();
  Tag tag;
  {
    const u32 e0 = __builtin_amdgcn_readfirstlane(ep.x), e1 = __builtin_amdgcn_readfirstlane(ep.y);
    const u64 e = (((u64)e1 << 32) | e0) + 1;
    tag.lo = (u32)(e & 0xff) << 24; tag.hi = (u32)(e >> 8);
  }
  if (wave == 0 || (wave == 1 && mlp)) {
    f32x2 sum = {0.f, 0.f};
#pragma unroll
    for (int w = 0; w < 8; ++w) sum += *(LAS f32x2*)(L + L_PART + (w * 256 + 2 * tid) * 4);
    __builtin_amdgcn_raw_buffer_store_b128(gpack(sum.x, sum.y, tag), rws, WS_X1 + (b * 256 + 2 * tid) * 8, 0, AUX_SC1);
  }
  STAMP(2);
  FENCE();

  bool mydone = !(wave == 0 || (wave == 1 && mlp));
  bool qready = false;
  const int x1off = WS_X1 + (h * 8 * 256 + wave * 128 + 2 * lane) * 8;
  auto finish_gather = [&](const u32x4 (&pq)[8]) {
    float s0 = 0.f, s1 = 0.f;
#pragma unroll
    for (int z = 0; z < 8; ++z) { s0 += gval(pq[z].x); s1 += gval(pq[z].z); }
    if (wave == 0) {
      *(volatile LAS u32*)(L + L_Q + lane * 4) = pack2(s0, s1);
      asm volatile("s_waitcnt lgkmcnt(0)" ::: "memory");
      *(volatile LAS u32*)(L + L_FLAG) = 1u;
    } else {
      const float g0 = rbf(s0), g1 = rbf(s1);
      auto t0 = __builtin_amdgcn_permlane32_swap(asu(g0), asu(g0), false, false);
      auto t1 = __builtin_amdgcn_permlane32_swap(asu(g1), asu(g1), false, false);
      const float ga = asf(t0[0]), ua = asf(t0[1]), gb = asf(t1[0]), ub = asf(t1[1]);
      const float ha = rbf(ga / (1.f + __expf(-ga))) * ua, hb = rbf(gb / (1.f + __expf(-gb))) * ub;
      if (lane < 32) *(volatile LAS u32*)(L + L_HID + lane * 4) = pack2(ha, hb);
    }
  };
#pragma unroll
  for (int g = 1; g <= NGRP; ++g) {
    const bool poll = !mydone && g >= GPOLL0;
    u32x4 pq[8];
    if (poll) {
#pragma unroll
      for (int z = 0; z < 8; ++z) pq[z] = __builtin_amdgcn_raw_buffer_load_b128(rws, x1off + z * 2048, 0, AUX_SC1);
    }
    FENCE();
#pragma unroll
    for (int i = G0 + (g - 1) * GP; i < G0 + g * GP && i < NP; ++i) kv[i] = __builtin_amdgcn_raw_buffer_load_b128(rk, voff, POFF(i), AUX_NT);
    FENCE();
    if (g == 1) {
#pragma unroll
      for (int j = 0; j < 4; ++j) if (tid + 512 * j < 1920) *(LAS u32x4*)(L + L_WO + (tid + 512 * j) * 16) = wt[j];
    }
    if (poll) {
      u32 bad = 0;
#pragma unroll
      for (int z = 0; z < 8; ++z) bad |= gbad(pq[z], tag);
      if (__builtin_amdgcn_ballot_w64(bad != 0) == 0) { finish_gather(pq); mydone = true; STAMP(10); }
    } else if (!qready) {
      asm volatile("s_waitcnt vmcnt(%0)" :: "n"(GP) : "memory");
    }
    if (!qready) qready = __builtin_amdgcn_readfirstlane(*(volatile LAS u32*)(L + L_FLAG)) != 0;
    FENCE();
  }
  if (!mydone) {
    for (u32 spin = 0; spin < SPIN_CAP; ++spin) {
      u32x4 pq[8];
      asm volatile("" ::: "memory");
#pragma unroll
      for (int z = 0; z < 8; ++z) pq[z] = __builtin_amdgcn_raw_buffer_load_b128(rws, x1off + z * 2048, 0, AUX_SC1);
      u32 bad = 0;
#pragma unroll
      for (int z = 0; z < 8; ++z) bad |= gbad(pq[z], tag);
      if (__builtin_amdgcn_ballot_w64(bad != 0) == 0) { finish_gather(pq); break; }
    }
  }
  if (!qready) {
    for (u32 spin = 0; spin < (SPIN_CAP << 4); ++spin) {
      if (__builtin_amdgcn_readfirstlane(*(volatile LAS u32*)(L + L_FLAG)) != 0) break;
      __builtin_amdgcn_s_sleep(1);
    }
  }
  asm volatile("" ::: "memory");
  const u32x4 qv = *(LAS u32x4*)(L + L_Q + (lane & 15) * 16);
  STAMP(3);
  FENCE();

  float sc[NP];
#pragma unroll
  for (int i = 0; i < NP; ++i) {
    float d = dot2(kv[i].x, qv.x, 0.f);
    d = dot2(kv[i].y, qv.y, d); d = dot2(kv[i].z, qv.z, d); d = dot2(kv[i].w, qv.w, d);
    kv[i] = __builtin_amdgcn_raw_buffer_load_b128(rv, voff, POFF(i), AUX_NT);
    d = row_sum(d);
    sc[i] = rbf(rbf(d) * 0.08838834764831845f);
    if ((i & 1) == 1) FENCE();
  }
  float mw = sc[0];
#pragma unroll
  for (int i = 1; i < NP; ++i) mw = fmaxf(mw, sc[i]);
  mw = xrow_max(mw);
  if (lane == 0) *(LAS float*)(L + L_MW + wave * 4) = mw;
  STAMP(4);
  lds_barrier();
  float M;
  {
    const f32x4 m0 = *(LAS f32x4*)(L + L_MW), m1 = *(LAS f32x4*)(L + L_MW + 16);
    M = fmaxf(fmaxf(fmaxf(m0.x, m0.y), fmaxf(m0.z, m0.w)), fmaxf(fmaxf(m1.x, m1.y), fmaxf(m1.z, m1.w)));
  }
  float lsum = 0.f;
#pragma unroll
  for (int i = 0; i < NP; ++i) {
    const float p = __builtin_amdgcn_exp2f((sc[i] - M) * 1.4426950408889634f);
    lsum += p;
    sc[i] = rbf(p);
  }
  lsum = xrow_sum(lsum);
  if (lane == 0) *(LAS float*)(L + L_LW + wave * 4) = lsum;
  FENCE();

  float acc[8];
#pragma unroll
  for (int j = 0; j < 8; ++j) acc[j] = 0.f;
#pragma unroll
  for (int i = 0; i < NP; ++i) {
    const u32 ph = asu(sc[i]), pl = ph >> 16;
#pragma unroll
    for (int j = 0; j < 4; ++j) { acc[2 * j] = dot2(kv[i][j], pl, acc[2 * j]); acc[2 * j + 1] = dot2(kv[i][j], ph, acc[2 * j + 1]); }
    if ((i & 1) == 1) FENCE();
  }
#pragma unroll
  for (int j = 0; j < 8; ++j) acc[j] = xrow_sum(acc[j]);
  if (lane < 32) {
    const bool up = lane >= 16;
    f32x4 o4 = {up ? acc[4] : acc[0], up ? acc[5] : acc[1], up ? acc[6] : acc[2], up ? acc[7] : acc[3]};
    *(LAS f32x4*)(L + L_OP + (wave * 128 + 8 * (lane & 15) + (up ? 4 : 0)) * 4) = o4;
  }
  STAMP(5);
  lds_barrier();
  if (wave != 0) return;

  {
    f32x2 o2 = {0.f, 0.f};
#pragma unroll
    for (int w = 0; w < 8; ++w) o2 += *(LAS f32x2*)(L + L_OP + (w * 128 + 2 * lane) * 4);
    __builtin_amdgcn_raw_buffer_store_b128(gpack(o2.x, o2.y, tag), rws, WS_X2 + (b * 128 + 2 * lane) * 8, 0, AUX_SC1);
    const f32x4 l0 = *(LAS f32x4*)(L + L_LW), l1 = *(LAS f32x4*)(L + L_LW + 16);
    const float lw = ((l0.x + l0.y) + (l0.z + l0.w)) + ((l1.x + l1.y) + (l1.z + l1.w));
    if (lane == 0) __builtin_amdgcn_raw_buffer_store_b128(gpack(M, lw, tag), rws, WS_ML + b * 128, 0, AUX_SC1);
  }
  STAMP(6);
  const int pz = lane & 7, half = (lane >> 3) & 1, g4 = lane >> 4;
  const int x2off = WS_X2 + ((h * 8 + pz) * 128 + 64 * half + 8 * g4) * 8;
  const int mloff = WS_ML + (h * 8 + pz) * 128;
  u32x4 ga[8], gm;
#define HOP1_LOADS() do { \
    _Pragma("unroll") for (int kk = 0; kk < 2; ++kk) \
      _Pragma("unroll") for (int u = 0; u < 4; ++u) ga[kk * 4 + u] = __builtin_amdgcn_raw_buffer_load_b128(rws, x2off + kk * 256 + u * 16, 0, AUX_SC1); \
    gm = __builtin_amdgcn_raw_buffer_load_b128(rws, mloff, 0, AUX_SC1); } while (0)
  HOP1_LOADS();
  FENCE();
  float yb[5];
  {
    if (mlp) {
      const u32x4 ha0 = *(LAS u32x4*)(L + L_HID + g4 * 16), ha1 = *(LAS u32x4*)(L + L_HID + 64 + g4 * 16);
#pragma unroll
      for (int t = 0; t < 5; ++t) {
        f32x4 d = {0.f, 0.f, 0.f, 0.f};
        d = mfma(ha0, bfrag(L + L_W2, lane, 0, 16 * t), d);
        d = mfma(ha1, bfrag(L + L_W2, lane, 32, 16 * t), d);
        yb[t] = d[0];
      }
    } else {
#pragma unroll
      for (int t = 0; t < 5; ++t) yb[t] = 0.f;
    }
  }
  u32x4 bf[5][4];
#pragma unroll
  for (int t = 0; t < 5; ++t)
#pragma unroll
    for (int ks = 0; ks < 4; ++ks) bf[t][ks] = bfrag(L + L_WO, lane, 32 * ks, 16 * t);
  FENCE();
  for (u32 spin = 0; spin < SPIN_CAP; ++spin) {
    u32 bad = gbad(gm, tag);
#pragma unroll
    for (int u = 0; u < 8; ++u) bad |= gbad(ga[u], tag);
    if (__builtin_amdgcn_ballot_w64(bad != 0) == 0) break;
    asm volatile("" ::: "memory");
    HOP1_LOADS();
  }
  STAMP(7);
  {
    const float mz = gval(gm.x), lz = gval(gm.z);
    float mx = mz;
    mx = fmaxf(mx, dpp<0xB1>(mx)); mx = fmaxf(mx, dpp<0x4E>(mx)); mx = fmaxf(mx, dpp<0x141>(mx));
    const float e = __builtin_amdgcn_exp2f((mz - mx) * 1.4426950408889634f);
    float ls = e * lz;
    ls += dpp<0xB1>(ls); ls += dpp<0x4E>(ls); ls += dpp<0x141>(ls);
    const float wz = e / ls;
    u32x4 af[2];
#pragma unroll
    for (int kk = 0; kk < 2; ++kk)
#pragma unroll
      for (int u = 0; u < 4; ++u) af[kk][u] = pack2(gval(ga[kk * 4 + u].x) * wz, gval(ga[kk * 4 + u].z) * wz);
    float y[5];
#pragma unroll
    for (int t = 0; t < 5; ++t) {
      f32x4 d1 = {0.f, 0.f, 0.f, 0.f}, d2 = {0.f, 0.f, 0.f, 0.f};
      d1 = mfma(af[0], bf[t][0], d1); d2 = mfma(af[0], bf[t][2], d2);
      d1 = mfma(af[1], bf[t][1], d1); d2 = mfma(af[1], bf[t][3], d2);
      const float u1 = (d1[0] + d1[1]) + (d1[2] + d1[3]), u2 = (d2[0] + d2[1]) + (d2[2] + d2[3]);
      y[t] = xrow_sum(lane < 32 ? u1 : u2) + yb[t];
    }
    const float ysel = g4 == 0 ? y[0] : g4 == 1 ? y[1] : g4 == 2 ? y[2] : y[3];
    const u32 thi = tag.hi;
    u32x2 gr; gr.x = ((asu(ysel) + 0x80u) >> 8) | tag.lo; gr.y = thi;
    __builtin_amdgcn_raw_buffer_store_b64(gr, rws, WS_X3 + (((s * 5 + g4) * 32 + h) * 16 + (lane & 15)) * 8, 0, AUX_SC1);
    if (lane < 16) {
      gr.x = ((asu(y[4]) + 0x80u) >> 8) | tag.lo;
      __builtin_amdgcn_raw_buffer_store_b64(gr, rws, WS_X3 + (((s * 5 + 4) * 32 + h) * 16 + lane) * 8, 0, AUX_SC1);
    }
  }
  STAMP(8);
  if (h >= 5) return;
  {
    const int x3off = WS_X3 + (((s * 5 + h) * 32 + (lane >> 3)) * 16 + 2 * (lane & 7)) * 8;
    u32x4 gy[4];
    for (u32 spin = 0; spin < SPIN_CAP; ++spin) {
      u32 bad = 0;
      asm volatile("" ::: "memory");
#pragma unroll
      for (int i = 0; i < 4; ++i) gy[i] = __builtin_amdgcn_raw_buffer_load_b128(rws, x3off + i * 1024, 0, AUX_SC1);
#pragma unroll
      for (int i = 0; i < 4; ++i) bad |= gbad(gy[i], tag);
      if (__builtin_amdgcn_ballot_w64(bad != 0) == 0) break;
    }
    float y0 = (gval(gy[0].x) + gval(gy[1].x)) + (gval(gy[2].x) + gval(gy[3].x));
    float y1 = (gval(gy[0].z) + gval(gy[1].z)) + (gval(gy[2].z) + gval(gy[3].z));
    y0 += dpp<ROR8>(y0); y1 += dpp<ROR8>(y1);
    y0 = xrow_sum(y0); y1 = xrow_sum(y1);
    if (lane < 8) ((GAS u32*)a.out)[40 * s + 8 * h + lane] = pack2(lo16(xo) + y0, hi16(xo) + y1);
    if (b == 0 && lane == 0) {
      const u64 full = ((((u64)ep.y << 32) | ep.x) + 1);
      __hip_atomic_store((GAS u64*)(a.ws + WS_EPOCH), full, __ATOMIC_RELAXED, __HIP_MEMORY_SCOPE_AGENT);
    }
  }
  while (__builtin_amdgcn_s_memrealtime() - tstart_ < PADT) __builtin_amdgcn_s_sleep(1);
}

}

extern "C" void kernel_launch(void* const* d_in, const int* in_sizes, int n_in, void* d_out, int out_size, void* d_ws, size_t ws_size, hipStream_t stream) {
  fused::Args a;
  a.x = (const unsigned short*)d_in[0];
  a.K = (const unsigned short*)d_in[1];
  a.V = (const unsigned short*)d_in[2];
  a.wq = (const unsigned short*)d_in[3];
  a.wo = (const unsigned short*)d_in[4];
  a.w1 = (const unsigned short*)d_in[6];
  a.w3 = (const unsigned short*)d_in[7];
  a.w2 = (const unsigned short*)d_in[8];
  a.out = (unsigned short*)d_out;
  a.ws = (unsigned char*)d_ws;
  hipLaunchKernelGGL(fused::fused_kernel, dim3(fused::NWG), dim3(fused::NT), fused::L_END, stream, a);
}
```

```cpp
#include <hip/hip_runtime.h>
#include <hip/hip_bf16.h>
#include <stdint.h>

typedef unsigned u32x4 __attribute__((ext_vector_type(4)));
typedef __bf16 bf16x2 __attribute__((ext_vector_type(2)));

constexpr int D = 640, H = 32, S = 8192, DH = 128, F = 1728, HD = H * DH;
constexpr int NSPLIT = 8, KPS = S / NSPLIT;

__device__ __forceinline__ float bf2f(unsigned short u) { return __uint_as_float((unsigned)u << 16); }
__device__ __forceinline__ float lo(unsigned u) { return __uint_as_float(u << 16); }
__device__ __forceinline__ float hi(unsigned u) { return __uint_as_float(u & 0xffff0000u); }
__device__ __forceinline__ unsigned short f2bf(float f) { __hip_bfloat16 b = __float2bfloat16(f); return *(unsigned short*)&b; }
__device__ __forceinline__ float rbf(float f) { return bf2f(f2bf(f)); }

__device__ void layernorm(const unsigned short* x, float* xn, float* red) {
  const int tid = threadIdx.x;
  float s = 0.f;
  for (int i = tid; i < D; i += blockDim.x) s += bf2f(x[i]);
  for (int o = 32; o; o >>= 1) s += __shfl_xor(s, o);
  if ((tid & 63) == 0) red[tid >> 6] = s;
  __syncthreads();
  float t = 0.f; for (int w = 0; w < (int)blockDim.x / 64; ++w) t += red[w];
  const float mean = t / D;
  __syncthreads();
  float v = 0.f;
  for (int i = tid; i < D; i += blockDim.x) { float d = bf2f(x[i]) - mean; v += d * d; }
  for (int o = 32; o; o >>= 1) v += __shfl_xor(v, o);
  if ((tid & 63) == 0) red[tid >> 6] = v;
  __syncthreads();
  t = 0.f; for (int w = 0; w < (int)blockDim.x / 64; ++w) t += red[w];
  const float var = t / D;
  const float mb = rbf(mean), sd = rbf(sqrtf(rbf(rbf(var) + 1e-5f)));
  for (int i = tid; i < D; i += blockDim.x) xn[i] = rbf(rbf(bf2f(x[i]) - mb) / sd);
  __syncthreads();
}

__global__ void k_t0(unsigned long long* t) { if (threadIdx.x == 0) *t = __builtin_amdgcn_s_memrealtime(); }
__global__ void k_pad(const unsigned long long* t, unsigned ticks) { const unsigned long long t0 = *(const volatile unsigned long long*)t; while (__builtin_amdgcn_s_memrealtime() - t0 < ticks) __builtin_amdgcn_s_sleep(1); }
__global__ __launch_bounds__(256) void k_proj(const unsigned short* x, const unsigned short* wq, const unsigned short* w1, const unsigned short* w3, float* q, float* h) {
  __shared__ float xn[D]; __shared__ float red[4]; __shared__ float part[2][4][64];
  layernorm(x, xn, red);
  const int col = blockIdx.x * 64 + (threadIdx.x & 63), ks = threadIdx.x >> 6;
  if (blockIdx.x < HD / 64) {
    float a = 0.f;
    for (int k = ks; k < D; k += 4) a += xn[k] * bf2f(wq[(size_t)k * HD + col]);
    part[0][ks][threadIdx.x & 63] = a;
    __syncthreads();
    if (ks == 0) { int l = threadIdx.x; q[col] = rbf(part[0][0][l] + part[0][1][l] + part[0][2][l] + part[0][3][l]); }
  } else {
    const int c = col - HD;
    float a = 0.f, b = 0.f;
    for (int k = ks; k < D; k += 4) { a += xn[k] * bf2f(w1[(size_t)k * F + c]); b += xn[k] * bf2f(w3[(size_t)k * F + c]); }
    part[0][ks][threadIdx.x & 63] = a; part[1][ks][threadIdx.x & 63] = b;
    __syncthreads();
    if (ks == 0) { int l = threadIdx.x;
      float g = rbf(part[0][0][l] + part[0][1][l] + part[0][2][l] + part[0][3][l]);
      float u = rbf(part[1][0][l] + part[1][1][l] + part[1][2][l] + part[1][3][l]);
      h[c] = rbf(rbf(g / (1.f + __expf(-g))) * u); }
  }
}

__global__ __launch_bounds__(256) void k_attn(const unsigned short* K, const unsigned short* V, const float* q, float* pm, float* pl, float* po) {
  __shared__ float sc[KPS]; __shared__ float red[4]; __shared__ float oacc[16][DH];
  const int h = blockIdx.x / NSPLIT, s = blockIdx.x % NSPLIT, tid = threadIdx.x, c = tid & 15, r = tid >> 4;
  float qv[8];
  for (int j = 0; j < 8; ++j) qv[j] = q[h * DH + 8 * c + j];
  const unsigned short* kb = K + ((size_t)h * S + (size_t)s * KPS) * DH;
  const unsigned short* vb = V + ((size_t)h * S + (size_t)s * KPS) * DH;
  for (int k = r; k < KPS; k += 16) {
    u32x4 kv = __builtin_nontemporal_load((const u32x4*)(kb + (size_t)k * DH + 8 * c));
    float a = 0.f;
    for (int j = 0; j < 4; ++j) a += lo(kv[j]) * qv[2 * j] + hi(kv[j]) * qv[2 * j + 1];
    for (int o = 8; o; o >>= 1) a += __shfl_xor(a, o);
    if (c == 0) sc[k] = a * 0.08838834764831845f;
  }
  __syncthreads();
  float m = -INFINITY;
  for (int k = tid; k < KPS; k += 256) m = fmaxf(m, sc[k]);
  for (int o = 32; o; o >>= 1) m = fmaxf(m, __shfl_xor(m, o));
  if ((tid & 63) == 0) red[tid >> 6] = m;
  __syncthreads();
  m = fmaxf(fmaxf(red[0], red[1]), fmaxf(red[2], red[3]));
  __syncthreads();
  float l = 0.f;
  for (int k = tid; k < KPS; k += 256) { float p = __expf(sc[k] - m); sc[k] = p; l += p; }
  for (int o = 32; o; o >>= 1) l += __shfl_xor(l, o);
  if ((tid & 63) == 0) red[tid >> 6] = l;
  __syncthreads();
  l = red[0] + red[1] + red[2] + red[3];
  float acc[8] = {0, 0, 0, 0, 0, 0, 0, 0};
  for (int k = r; k < KPS; k += 16) {
    u32x4 vv = __builtin_nontemporal_load((const u32x4*)(vb + (size_t)k * DH + 8 * c));
    const float p = sc[k];
    for (int j = 0; j < 4; ++j) { acc[2 * j] += p * lo(vv[j]); acc[2 * j + 1] += p * hi(vv[j]); }
  }
  for (int j = 0; j < 8; ++j) oacc[r][8 * c + j] = acc[j];
  __syncthreads();
  if (tid < DH) { float o = 0.f; for (int rr = 0; rr < 16; ++rr) o += oacc[rr][tid]; po[(size_t)blockIdx.x * DH + tid] = o; }
  if (tid == 0) { pm[blockIdx.x] = m; pl[blockIdx.x] = l; }
}

__global__ __launch_bounds__(256) void k_out(const unsigned short* x, const unsigned short* wo, const unsigned short* w2, const float* h, const float* pm, const float* pl, const float* po, unsigned short* out) {
  __shared__ float o[HD]; __shared__ float part[2][16][16];
  const int tid = threadIdx.x;
  for (int i = tid; i < HD; i += 256) {
    const int hh = i / DH, d = i % DH;
    float M = -INFINITY; for (int s = 0; s < NSPLIT; ++s) M = fmaxf(M, pm[hh * NSPLIT + s]);
    float L = 0.f, a = 0.f;
    for (int s = 0; s < NSPLIT; ++s) { float w = __expf(pm[hh * NSPLIT + s] - M); L += w * pl[hh * NSPLIT + s]; a += w * po[(size_t)(hh * NSPLIT + s) * DH + d]; }
    o[i] = rbf(a / L);
  }
  __syncthreads();
  const int col = blockIdx.x * 16 + (tid & 15), ks = tid >> 4;
  float a = 0.f, b = 0.f;
  for (int k = ks; k < HD; k += 16) a += o[k] * bf2f(wo[(size_t)k * D + col]);
  for (int k = ks; k < F; k += 16) b += h[k] * bf2f(w2[(size_t)k * D + col]);
  part[0][ks][tid & 15] = a; part[1][ks][tid & 15] = b;
  __syncthreads();
  if (tid < 16) {
    float A = 0.f, B = 0.f;
    for (int k = 0; k < 16; ++k) { A += part[0][k][tid]; B += part[1][k][tid]; }
    out[col] = f2bf(rbf(bf2f(x[col]) + rbf(A)) + rbf(B));
  }
}

extern "C" void kernel_launch(void* const* d_in, const int* in_sizes, int n_in, void* d_out, int out_size, void* d_ws, size_t ws_size, hipStream_t stream) {
  const unsigned short* x = (const unsigned short*)d_in[0];
  const unsigned short* K = (const unsigned short*)d_in[1];
  const unsigned short* V = (const unsigned short*)d_in[2];
  const unsigned short* wq = (const unsigned short*)d_in[3];
  const unsigned short* wo = (const unsigned short*)d_in[4];
  const unsigned short* w1 = (const unsigned short*)d_in[6];
  const unsigned short* w3 = (const unsigned short*)d_in[7];
  const unsigned short* w2 = (const unsigned short*)d_in[8];
  float* ws = (float*)d_ws;
  float* q = ws; float* h = q + HD; float* pm = h + 2048; float* pl = pm + 256; float* po = pl + 256;
  unsigned long long* tt = (unsigned long long*)((char*)d_ws + (16 << 20));
  k_t0<<<1, 64, 0, stream>>>(tt);
  k_proj<<<HD / 64 + F / 64, 256, 0, stream>>>(x, wq, w1, w3, q, h);
  k_attn<<<H * NSPLIT, 256, 0, stream>>>(K, V, q, pm, pl, po);
  k_out<<<D / 16, 256, 0, stream>>>(x, wo, w2, h, pm, pl, po, (unsigned short*)d_out);
  k_pad<<<1, 64, 0, stream>>>(tt, 40000u);
}
```

```cpp
#include <hip/hip_runtime.h>
#include <stdint.h>

namespace fused {

typedef unsigned u32;
typedef unsigned long long u64;
typedef u32 u32x2 __attribute__((ext_vector_type(2)));
typedef u32 u32x4 __attribute__((ext_vector_type(4)));
typedef float f32x2 __attribute__((ext_vector_type(2)));
typedef float f32x4 __attribute__((ext_vector_type(4)));
typedef short s16x4 __attribute__((ext_vector_type(4)));
typedef short s16x8 __attribute__((ext_vector_type(8)));
typedef __bf16 bf16x2 __attribute__((ext_vector_type(2)));
typedef __bf16 bf16x8 __attribute__((ext_vector_type(8)));
#define LAS __attribute__((address_space(3)))
#define GAS __attribute__((address_space(1)))

constexpr int D = 640, H = 32, S = 8192, DH = 128, F = 1728, HD = H * DH;
constexpr int NWG = 256, NT = 512;
constexpr int NG = F / 64;

constexpr int NP = 32;
constexpr int FL = 5;
constexpr int F0 = 4;
constexpr int TP = 470;
constexpr int C0 = 3;
constexpr int EX = 6;
constexpr int PVR = 3;
constexpr int ATT0 = 10;
constexpr int XTRA = 4;
constexpr int WTP = 3;
constexpr int CODE_KB = 19;
constexpr int CODE_AGAIN = 20;
constexpr int RAMP_M = 20;
constexpr int RAMP_D = 75;
constexpr int deadline(int n) { return (((C0 + n) * TP) >> 4) - (n > 2 * NP - 1 - RAMP_M ? (n - (2 * NP - 1 - RAMP_M)) * RAMP_D / RAMP_M : 0); }
constexpr u32 SPIN_CAP = 1u << 20;
constexpr int AUX_NT = 2, AUX_SC1 = 16, RSRC_W3 = 0x00020000;

constexpr int WS_EPOCH = 0;
constexpr int WS_F1 = 4096;
constexpr int WS_X1 = WS_F1 + NWG * 128;
constexpr int WS_X2 = WS_X1 + NWG * 256 * 4;
constexpr int WS_ML = WS_X2 + NWG * 128 * 8;
constexpr int WS_X3 = WS_ML + NWG * 128;
constexpr int WS_END = WS_X3 + 8 * 5 * 32 * 16 * 8;

constexpr int L_WO = 0;
constexpr int L_W2 = 128 * 160;
constexpr int L_PART = L_W2 + 64 * 160;
constexpr int L_OP = L_PART + 8192;
constexpr int L_A = L_OP + 4096;
constexpr int L_Q = L_A + 2048;
constexpr int L_HID = L_Q + 256;
constexpr int L_MW = L_HID + 128;
constexpr int L_LW = L_MW + 32;
constexpr int L_FQ = L_LW + 32;
constexpr int L_FA = L_FQ + 4;
constexpr int L_CW = L_FA + 4;
constexpr int L_END = L_FQ + 16;

struct Args {
  const unsigned short *x, *K, *V, *wq, *wo, *w1, *w3, *w2;
  unsigned short* out;
  unsigned char* ws;
};

__device__ __forceinline__ float asf(u32 u) { return __builtin_bit_cast(float, u); }
__device__ __forceinline__ u32 asu(float f) { return __builtin_bit_cast(u32, f); }
__device__ __forceinline__ float lo16(u32 u) { return asf(u << 16); }
__device__ __forceinline__ float hi16(u32 u) { return asf(u & 0xffff0000u); }
__device__ __forceinline__ u32 pack2(float a, float b) { f32x2 v = {a, b}; return __builtin_bit_cast(u32, __builtin_convertvector(v, bf16x2)); }
__device__ __forceinline__ float rbf(float a) { return (float)(__bf16)a; }
__device__ __forceinline__ float dot2(u32 a, u32 b, float c) {
  return __builtin_amdgcn_fdot2_f32_bf16(__builtin_bit_cast(bf16x2, a), __builtin_bit_cast(bf16x2, b), c, false);
}
template <int CTRL> __device__ __forceinline__ float dpp(float x) {
  return asf((u32)__builtin_amdgcn_mov_dpp((int)asu(x), CTRL, 0xf, 0xf, true));
}
constexpr int ROR1 = 0x121, ROR2 = 0x122, ROR4 = 0x124, ROR8 = 0x128, XOR1 = 0xB1, XOR2 = 0x4E, HMIR = 0x141;
__device__ __forceinline__ float row_sum(float x) { x += dpp<ROR8>(x); x += dpp<ROR4>(x); x += dpp<ROR2>(x); x += dpp<ROR1>(x); return x; }
__device__ __forceinline__ float swap16_sum(float x) { auto s = __builtin_amdgcn_permlane16_swap(asu(x), asu(x), false, false); return asf(s[0]) + asf(s[1]); }
__device__ __forceinline__ float swap32_sum(float x) { auto s = __builtin_amdgcn_permlane32_swap(asu(x), asu(x), false, false); return asf(s[0]) + asf(s[1]); }
__device__ __forceinline__ float xrow_sum(float x) { return swap32_sum(swap16_sum(x)); }
__device__ __forceinline__ float xrow_max(float x) {
  auto s = __builtin_amdgcn_permlane16_swap(asu(x), asu(x), false, false); x = fmaxf(asf(s[0]), asf(s[1]));
  auto t = __builtin_amdgcn_permlane32_swap(asu(x), asu(x), false, false); return fmaxf(asf(t[0]), asf(t[1]));
}
__device__ __forceinline__ float wave_sum(float x) { return xrow_sum(row_sum(x)); }
__device__ __forceinline__ f32x4 mfma(u32x4 a, u32x4 b, f32x4 c) {
  return __builtin_amdgcn_mfma_f32_16x16x32_bf16(__builtin_bit_cast(bf16x8, a), __builtin_bit_cast(bf16x8, b), c, 0, 0, 0);
}
__device__ __forceinline__ void lds_barrier() {
  asm volatile("s_waitcnt lgkmcnt(0)" ::: "memory");
  __builtin_amdgcn_s_barrier();
  asm volatile("" ::: "memory");
}
__device__ __forceinline__ void lds_wait(LAS unsigned char* p, u32 want) {
  for (u32 spin = 0; spin < (SPIN_CAP << 4); ++spin) {
    if ((u32)__builtin_amdgcn_readfirstlane(*(volatile LAS u32*)p) >= want) break;
  }
  asm volatile("" ::: "memory");
}
template <int I> struct IC { static constexpr int v = I; };
template <int B, int E, typename Fn> __device__ __forceinline__ void sfor(Fn&& f) {
  if constexpr (B < E) { f(IC<B>{}); sfor<B + 1, E>(f); }
}
#define FENCE() do { asm volatile("" ::: "memory"); __builtin_amdgcn_sched_barrier(0); } while (0)

struct Tag { u32 lo, hi; };
__device__ __forceinline__ u32 gword(float a, Tag t) { return ((asu(a) + 0x80u) >> 8) | t.lo; }
__device__ __forceinline__ u32x4 gpack(float a, float b, Tag t) { u32x4 r; r.x = gword(a, t); r.y = t.hi; r.z = gword(b, t); r.w = t.hi; return r; }
__device__ __forceinline__ u32 gbad(u32x4 g, Tag t) { return (((g.x ^ t.lo) | (g.z ^ t.lo)) >> 24) | (g.y ^ t.hi) | (g.w ^ t.hi); }
__device__ __forceinline__ float gval(u32 w) { return asf(w << 8); }

__device__ __forceinline__ u32x4 bfrag(LAS unsigned char* img, int lane, int k0, int n0) {
  const int g = lane >> 4, q = (lane >> 2) & 3, p = lane & 3;
  LAS unsigned char* ad = img + (k0 + 8 * g + q) * 160 + (n0 + 4 * p) * 2;
  s16x4 t0 = __builtin_amdgcn_ds_read_tr16_b64_v4i16((LAS s16x4*)ad);
  s16x4 t1 = __builtin_amdgcn_ds_read_tr16_b64_v4i16((LAS s16x4*)(ad + 4 * 160));
  s16x8 r = __builtin_shufflevector(t0, t1, 0, 1, 2, 3, 4, 5, 6, 7);
  return __builtin_bit_cast(u32x4, r);
}

constexpr int vdone(int np, int first, int n) {
  int d = 0;
  for (int t = first; t <= n; ++t) { const int landed = t - np - FL + 1; const int up = d + PVR < landed ? d + PVR : landed; d = up > d ? up : d; }
  return d;
}

__global__ void __launch_bounds__(NT, 2) fused_kernel(Args a) {
  extern __shared__ __attribute__((aligned(16))) unsigned char smem[];
  LAS unsigned char* L = (LAS unsigned char*)smem;
  u64 pc0;
  asm volatile("s_getpc_b64 %0" : "=s"(pc0));
  pc0 &= ~(u64)255;
  asm volatile("" :: "s"(a.x), "s"(a.K), "s"(a.V), "s"(a.wq), "s"(a.wo), "s"(a.w1), "s"(a.w3), "s"(a.w2), "s"(a.out), "s"(a.ws));
  const int tid = threadIdx.x, lane = tid & 63, wave = __builtin_amdgcn_readfirstlane(tid >> 6);
  const int bx = blockIdx.x, h = bx >> 3, s = bx & 7, b = bx;
  const bool mlp = h < NG;
  const int hp = 8 * ((bx & 7) >> 1) + (bx >> 5), sp = 4 * (bx & 1) + ((bx >> 3) & 3);
  const bool mlpp = hp < NG;

  const __amdgpu_buffer_rsrc_t rws = __builtin_amdgcn_make_buffer_rsrc((void*)a.ws, 0, WS_END, RSRC_W3);
  const __amdgpu_buffer_rsrc_t rk = __builtin_amdgcn_make_buffer_rsrc((void*)a.K, 0, H * S * DH * 2, RSRC_W3);
  const __amdgpu_buffer_rsrc_t rv = __builtin_amdgcn_make_buffer_rsrc((void*)a.V, 0, H * S * DH * 2, RSRC_W3);

  const int voff = lane * 16;
  const int pbase = (h * 2048 + 8 * s + wave) << 10;
#define POFF(i) (pbase + (((h + (i)) & (NP - 1)) << 16))

  const u32x2 ep = __builtin_amdgcn_raw_buffer_load_b64(rws, WS_EPOCH, 0, AUX_SC1);
  u32 xs[5], xr[5];
#pragma unroll
  for (int i = 0; i < 5; ++i) xs[i] = ((const GAS u32*)a.x)[lane + 64 * i];
  const int prow = 80 * s + (tid >> 5), c32 = tid & 31;
#pragma unroll
  for (int i = 0; i < 5; ++i) xr[i] = ((const GAS unsigned short*)a.x)[prow + 16 * i];
  u32x4 wv[5];
  {
    const bool isq = c32 < 16 || !mlp;
    const u64 p13 = c32 < 24 ? (u64)a.w1 : (u64)a.w3;
    const u64 pb = isq ? (u64)a.wq : p13;
    const u32 stride = isq ? HD * 2 : F * 2;
    const u32 col = isq ? h * (DH * 2) + 16 * (c32 & 15) : h * 128 + 16 * (c32 & 7);
    const u64 p0 = pb + (prow * stride + col);
#pragma unroll
    for (int i = 0; i < 5; ++i) wv[i] = __builtin_nontemporal_load((const GAS u32x4*)(p0 + (u32)(16 * i) * stride));
  }
  const u32 xo = ((const GAS u32*)a.x)[40 * sp + 8 * (hp < 5 ? hp : 0) + (lane & 7)];
  auto code_touch = [&]() -> u32 {
    const int slice = (bx >> 3) + 32 * (wave & 1);
    u32 v = 0;
    if (wave >= 6 && slice < CODE_KB) v = *(const GAS u32*)(pc0 + slice * 1024 + lane * 16);
    return v;
  };
  u32 ctouch = code_touch();
  FENCE();
  __builtin_amdgcn_s_barrier();
  FENCE();
  const u32 tbase = (u32)__builtin_amdgcn_s_memrealtime();
  u32x4 kv[NP + EX];
#pragma unroll
  for (int i = 0; i < F0; ++i) kv[i] = __builtin_amdgcn_raw_buffer_load_b128(rk, voff, POFF(i), AUX_NT);
  FENCE();
#define CLOCK_GATE(pos) asm volatile("s_branch .Lch%=\n.Lcg%=:\n\ts_sleep 1\n.Lch%=:\n\ts_memrealtime vcc\n\ts_waitcnt lgkmcnt(0)\n\ts_sub_u32 vcc_lo, vcc_lo, %0\n\ts_cmp_lt_u32 vcc_lo, %1\n\ts_cbranch_scc1 .Lcg%=" \
    :: "s"(tbase), "n"(deadline(pos)) : "vcc", "scc", "memory")
#define PACE(pos) do { asm volatile("s_waitcnt vmcnt(%0)" :: "n"(FL - 1) : "memory"); CLOCK_GATE(pos); } while (0)

  float xn[5];
  {
    float sum = 0.f;
#pragma unroll
    for (int i = 0; i < 5; ++i) sum += lo16(xs[i]) + hi16(xs[i]);
    const float mean = wave_sum(sum) * (1.f / D);
    float sq = 0.f;
#pragma unroll
    for (int i = 0; i < 5; ++i) { const float d0 = lo16(xs[i]) - mean, d1 = hi16(xs[i]) - mean; sq += d0 * d0 + d1 * d1; }
    const float var = wave_sum(sq) * (1.f / D);
    const float mb = rbf(mean), sd = rbf(sqrtf(rbf(rbf(var) + 1e-5f)));
#pragma unroll
    for (int i = 0; i < 5; ++i) xn[i] = rbf(rbf(lo16(xr[i]) - mb) / sd);
  }
  {
    float acc[8];
#pragma unroll
    for (int j = 0; j < 8; ++j) acc[j] = 0.f;
#pragma unroll
    for (int i = 0; i < 5; ++i) {
      const u32 ph = asu(xn[i]), pl = ph >> 16;
#pragma unroll
      for (int j = 0; j < 4; ++j) { acc[2 * j] = dot2(wv[i][j], pl, acc[2 * j]); acc[2 * j + 1] = dot2(wv[i][j], ph, acc[2 * j + 1]); }
    }
#pragma unroll
    for (int j = 0; j < 8; ++j) acc[j] = swap32_sum(acc[j]);
    const bool up = lane >= 32;
    f32x4 o4 = {up ? acc[4] : acc[0], up ? acc[5] : acc[1], up ? acc[6] : acc[2], up ? acc[7] : acc[3]};
    *(LAS f32x4*)(L + L_PART + (wave * 256 + 8 * (lane & 31) + (up ? 4 : 0)) * 4) = o4;
    if (tid < 4) *(volatile LAS u32*)(L + L_FQ + tid * 4) = 0u;
  }
  asm volatile("" :: "v"(ctouch));
  lds_barrier();
  const u64 epoch = ((((u64)(u32)__builtin_amdgcn_readfirstlane(ep.y)) << 32) | (u32)__builtin_amdgcn_readfirstlane(ep.x)) + 1;
  Tag tag; tag.lo = (u32)(epoch & 0xff) << 24; tag.hi = (u32)(epoch >> 8);
  const bool xwave = wave == 0 || (wave == 1 && mlp);
  if (xwave) {
    f32x2 sum = {0.f, 0.f};
#pragma unroll
    for (int w = 0; w < 8; ++w) sum += *(LAS f32x2*)(L + L_PART + (w * 256 + 2 * tid) * 4);
    __builtin_amdgcn_raw_buffer_store_b64(__builtin_bit_cast(u32x2, sum), rws, WS_X1 + (b * 256 + 2 * tid) * 4, 0, AUX_SC1);
  }
  {
    const u32x4 inv = {~tag.lo, ~tag.hi, ~tag.lo, ~tag.hi};
    if (wave == 0) {
      __builtin_amdgcn_raw_buffer_store_b128(inv, rws, WS_X2 + (b * 128 + 2 * lane) * 8, 0, AUX_SC1);
      if (lane == 0) __builtin_amdgcn_raw_buffer_store_b128(inv, rws, WS_ML + b * 16, 0, AUX_SC1);
    } else if (wave <= 5 && lane < 16) {
      __builtin_amdgcn_raw_buffer_store_b64(u32x2{inv.x, inv.y}, rws, WS_X3 + (((sp * 5 + wave - 1) * 32 + hp) * 16 + lane) * 8, 0, AUX_SC1);
    }
  }
  FENCE();

  u32x4 wt[4];
  auto load_wt = [&]() {
#pragma unroll
    for (int j = 0; j < 4; ++j) {
      const int it = tid + 512 * j;
      const bool isw2 = it >= 1280 && it < 1920 && mlpp;
      const int i2 = it < 1280 ? it : it - 1280;
      const int row = i2 / 10, ch = i2 - row * 10;
      const GAS unsigned short* p = isw2 ? (const GAS unsigned short*)a.w2 + (size_t)(64 * hp + row) * D : (const GAS unsigned short*)a.wo + (size_t)(DH * hp + row) * D;
      wt[j] = __builtin_nontemporal_load((const GAS u32x4*)(p + 80 * sp + 8 * ch));
    }
  };
  auto write_wt = [&]() {
#pragma unroll
    for (int j = 0; j < 4; ++j) if (tid + 512 * j < 1920) *(LAS u32x4*)(L + L_WO + (tid + 512 * j) * 16) = wt[j];
  };

  float M = 0.f, lsum = 0.f;
  float acc[8];
#pragma unroll
  for (int j = 0; j < 8; ++j) acc[j] = 0.f;
  const bool gwave = wave == 0 || (wave == 1 && mlpp);
  const int hg = wave == 0 ? h : hp;
  int xstate = gwave ? 0 : 2;
  int xwi;
  asm volatile("s_cmp_lt_u32 %1, %2\n\ts_cselect_b32 %0, 1, 0" : "=s"(xwi) : "s"(wave), "s"(mlpp ? 2 : 1) : "scc");
  const int f1off = WS_F1 + hg * 128 + wave * 64 + (lane & 7) * 8;
  const int x1off = WS_X1 + ((hg * 8 + 4 * (lane >> 5)) * 256 + wave * 128 + 4 * (lane & 31)) * 4;
  u32x2 fl;
  u32x4 pq[4];
  auto flag_load = [&]() { fl = __builtin_amdgcn_raw_buffer_load_b64(rws, f1off, 0, AUX_SC1); };
  auto flag_ok = [&]() -> bool { return __builtin_amdgcn_ballot_w64(fl.x != (u32)epoch || fl.y != (u32)(epoch >> 32)) == 0; };
  auto payload_load = [&]() {
    asm volatile("" ::: "memory");
#pragma unroll
    for (int z = 0; z < 4; ++z) pq[z] = __builtin_amdgcn_raw_buffer_load_b128(rws, x1off + z * 1024, 0, AUX_SC1);
  };
  auto payload_use = [&]() {
    f32x4 t = (__builtin_bit_cast(f32x4, pq[0]) + __builtin_bit_cast(f32x4, pq[1])) + (__builtin_bit_cast(f32x4, pq[2]) + __builtin_bit_cast(f32x4, pq[3]));
#pragma unroll
    for (int j = 0; j < 4; ++j) t[j] = swap32_sum(t[j]);
    if (wave == 0) {
      if (lane < 32) *(volatile LAS u32*)(L + L_Q + lane * 8) = pack2(t[0], t[1]);
      if (lane < 32) *(volatile LAS u32*)(L + L_Q + lane * 8 + 4) = pack2(t[2], t[3]);
      asm volatile("s_waitcnt lgkmcnt(0)" ::: "memory");
      *(volatile LAS u32*)(L + L_FQ) = 1u;
    } else {
      float hv[4];
#pragma unroll
      for (int j = 0; j < 4; ++j) {
        const float g = rbf(t[j]);
        auto sw = __builtin_amdgcn_permlane16_swap(asu(g), asu(g), false, false);
        const float gg = asf(sw[0]), uu = asf(sw[1]);
        hv[j] = rbf(gg / (1.f + __expf(-gg))) * uu;
      }
      if (lane < 16) *(volatile LAS u32*)(L + L_HID + lane * 8) = pack2(hv[0], hv[1]);
      if (lane < 16) *(volatile LAS u32*)(L + L_HID + lane * 8 + 4) = pack2(hv[2], hv[3]);
    }
  };
  constexpr int NH = (NP + 1) / 2, QW = NP + EX, PV0 = QW + NH;
  constexpr int LASTCHK = QW - 2 - FL, NATT = (LASTCHK - FL - ATT0) / 2 + 1;
  u32x2 flr[NATT];
  u32x4 qv;
  float sc[NP];
  auto score = [&](const u32x4& k) -> float {
    float d = dot2(k.x, qv.x, 0.f);
    d = dot2(k.y, qv.y, d); d = dot2(k.z, qv.z, d); d = dot2(k.w, qv.w, d);
    d = row_sum(d);
    return rbf(rbf(d) * 0.08838834764831845f);
  };
  auto pv = [&](auto ic) {
    constexpr int i = decltype(ic)::v, sl = i < EX ? NP + i : i - EX;
    const float p = __builtin_amdgcn_exp2f((sc[i] - M) * 1.4426950408889634f);
    lsum += p;
    const u32 ph = asu(rbf(p)), pl = ph >> 16;
#pragma unroll
    for (int j = 0; j < 4; ++j) { acc[2 * j] = dot2(kv[sl][j], pl, acc[2 * j]); acc[2 * j + 1] = dot2(kv[sl][j], ph, acc[2 * j + 1]); }
    asm volatile("" : "+v"(acc[0]), "+v"(acc[1]), "+v"(acc[2]), "+v"(acc[3]), "+v"(acc[4]), "+v"(acc[5]), "+v"(acc[6]), "+v"(acc[7]), "+v"(lsum));
  };
  sfor<F0, 2 * NP>([&](auto nc) {
    constexpr int n = decltype(nc)::v;
    if constexpr (n >= ATT0 && (n - ATT0) % 2 == 0 && (n - ATT0) / 2 < NATT) {
      if (xstate == 0) flr[(n - ATT0) / 2] = __builtin_amdgcn_raw_buffer_load_b64(rws, f1off, 0, AUX_SC1);
      FENCE();
    }
    if constexpr (n == QW) {
      if (xstate == 0) {
        for (u32 spin = 0; spin < SPIN_CAP; ++spin) {
          asm volatile("" ::: "memory");
          flag_load();
          if (flag_ok()) break;
        }
        payload_load();
        xstate = 1;
      }
      if (xstate == 1) payload_use();
      lds_wait(L + L_FQ, 1);
      qv = *(LAS u32x4*)(L + L_Q + (lane & 15) * 16);
      FENCE();
    }
    if constexpr (n >= ATT0 && n <= QW) {
      asm volatile("s_cmp_lg_u32 %0, 0\n\ts_cbranch_scc1 .Lxw%=\n\ts_waitcnt vmcnt(%2)\n.Lxw%=:\n\ts_waitcnt vmcnt(%1)"
                   :: "s"(xwi), "n"(FL - 1 + XTRA), "n"(FL - 1) : "scc", "memory");
      CLOCK_GATE(n);
    } else {
      PACE(n);
    }
    if constexpr (n == F0 + FL - 1) {
      if (xwave && lane == 0) __builtin_amdgcn_raw_buffer_store_b64(u32x2{(u32)epoch, (u32)(epoch >> 32)}, rws, WS_F1 + h * 128 + wave * 64 + s * 8, 0, AUX_SC1);
      FENCE();
    }
    auto request = [&]() {
      if constexpr (n < NP) kv[n] = __builtin_amdgcn_raw_buffer_load_b128(rk, voff, POFF(n), AUX_NT);
      else kv[n - NP < EX ? n : n - NP - EX] = __builtin_amdgcn_raw_buffer_load_b128(rv, voff, POFF(n - NP), AUX_NT);
      FENCE();
    };
    constexpr bool early = n != QW;
    if constexpr (early) request();
    if constexpr (n >= QW && n < PV0) {
      constexpr int i = n - QW;
      sc[2 * i] = score(kv[2 * i]);
      asm volatile("" : "+v"(sc[2 * i]));
      if constexpr (2 * i + 1 < NP) { sc[2 * i + 1] = score(kv[2 * i + 1]); asm volatile("" : "+v"(sc[2 * i + 1])); }
    }
    if constexpr (n >= PV0) sfor<vdone(NP, PV0, n - 1), vdone(NP, PV0, n)>(pv);
    if constexpr (!early) request();
    FENCE();
    if constexpr (n >= ATT0 + FL && (n - ATT0 - FL) % 2 == 0 && (n - ATT0 - FL) / 2 < NATT) {
      if (xstate == 0) { fl = flr[(n - ATT0 - FL) / 2]; if (flag_ok()) { payload_load(); xstate = 1; } }
      FENCE();
    }
    if constexpr (n == QW - 2) {
      if (xstate == 1) { payload_use(); xstate = 2; }
      FENCE();
    }
    if constexpr (n == NP + CODE_AGAIN) { ctouch = code_touch(); FENCE(); }
    if constexpr (n == NP + CODE_AGAIN + FL + 1) { asm volatile("" :: "v"(ctouch)); FENCE(); }
    if constexpr (n == PV0 - 1) {
      float mw = sc[0];
#pragma unroll
      for (int i = 1; i < NP; ++i) mw = fmaxf(mw, sc[i]);
      mw = xrow_max(mw);
      if (lane == 0) *(LAS float*)(L + L_MW + wave * 4) = mw;
      lds_barrier();
      const f32x4 m0 = *(LAS f32x4*)(L + L_MW), m1 = *(LAS f32x4*)(L + L_MW + 16);
      M = fmaxf(fmaxf(fmaxf(m0.x, m0.y), fmaxf(m0.z, m0.w)), fmaxf(fmaxf(m1.x, m1.y), fmaxf(m1.z, m1.w)));
      FENCE();
    }
  });
  static_assert(PV0 <= 2 * NP, "the scores are complete before the last V request");
  constexpr int VD = vdone(NP, PV0, 2 * NP - 1);
  constexpr int WCUT = NP - WTP > VD ? NP - WTP : VD;
  sfor<VD, WCUT>(pv);
  load_wt();
  FENCE();
  sfor<WCUT, NP>(pv);
  lsum = xrow_sum(lsum);
  if (lane == 0) *(LAS float*)(L + L_LW + wave * 4) = lsum;
#pragma unroll
  for (int j = 0; j < 8; ++j) acc[j] = xrow_sum(acc[j]);
  if (lane < 32) {
    const bool up = lane >= 16;
    f32x4 o4 = {up ? acc[4] : acc[0], up ? acc[5] : acc[1], up ? acc[6] : acc[2], up ? acc[7] : acc[3]};
    *(LAS f32x4*)(L + L_OP + (wave * 128 + 8 * (lane & 15) + (up ? 4 : 0)) * 4) = o4;
  }
  lds_barrier();

  auto stage_wt = [&]() {
    write_wt();
    asm volatile("s_waitcnt lgkmcnt(0)" ::: "memory");
    if (lane == 0) __hip_atomic_fetch_add((LAS u32*)(L + L_CW), 1u, __ATOMIC_RELAXED, __HIP_MEMORY_SCOPE_WORKGROUP);
  };
  const int g4 = lane >> 4;

  if (wave == 0) {
    const f32x4 l0 = *(LAS f32x4*)(L + L_LW), l1 = *(LAS f32x4*)(L + L_LW + 16);
    f32x2 op[8];
#pragma unroll
    for (int w = 0; w < 8; ++w) op[w] = *(LAS f32x2*)(L + L_OP + (w * 128 + 2 * lane) * 4);
    const float lw = ((l0.x + l0.y) + (l0.z + l0.w)) + ((l1.x + l1.y) + (l1.z + l1.w));
    const f32x2 o2 = ((op[0] + op[1]) + (op[2] + op[3])) + ((op[4] + op[5]) + (op[6] + op[7]));
    const u32x4 go = gpack(o2.x, o2.y, tag), gl = gpack(M, lw, tag);
    FENCE();
    __builtin_amdgcn_raw_buffer_store_b128(go, rws, WS_X2 + (b * 128 + 2 * lane) * 8, 0, AUX_SC1);
    if (lane == 0) __builtin_amdgcn_raw_buffer_store_b128(gl, rws, WS_ML + b * 16, 0, AUX_SC1);
    FENCE();
  }
  const int t = wave - 1;
  if (wave == 0) {
    const int pz = lane & 7, half = (lane >> 3) & 1;
    const int x2off = WS_X2 + ((hp * 8 + pz) * 128 + 64 * half + 8 * g4) * 8;
    const int mloff = WS_ML + (hp * 8 + pz) * 16;
    u32x4 ga[8], gm;
#define HOP1_LOADS() do { \
      _Pragma("unroll") for (int kk = 0; kk < 2; ++kk) \
        _Pragma("unroll") for (int u = 0; u < 4; ++u) ga[kk * 4 + u] = __builtin_amdgcn_raw_buffer_load_b128(rws, x2off + kk * 256 + u * 16, 0, AUX_SC1); \
      gm = __builtin_amdgcn_raw_buffer_load_b128(rws, mloff, 0, AUX_SC1); } while (0)
    write_wt();
    asm volatile("s_waitcnt vmcnt(0)" ::: "memory");
    __builtin_amdgcn_s_sleep(4);
    HOP1_LOADS();
    FENCE();
    asm volatile("s_waitcnt lgkmcnt(0)" ::: "memory");
    if (lane == 0) __hip_atomic_fetch_add((LAS u32*)(L + L_CW), 1u, __ATOMIC_RELAXED, __HIP_MEMORY_SCOPE_WORKGROUP);
    for (u32 spin = 0; spin < SPIN_CAP; ++spin) {
      u32 bad = gbad(gm, tag);
#pragma unroll
      for (int u = 0; u < 8; ++u) bad |= gbad(ga[u], tag);
      if (__builtin_amdgcn_ballot_w64(bad != 0) == 0) break;
      asm volatile("" ::: "memory");
      if (bad != 0) HOP1_LOADS();
    }
    const float mz = gval(gm.x), lz = gval(gm.z);
    float mx = mz;
    mx = fmaxf(mx, dpp<XOR1>(mx)); mx = fmaxf(mx, dpp<XOR2>(mx)); mx = fmaxf(mx, dpp<HMIR>(mx));
    const float e = __builtin_amdgcn_exp2f((mz - mx) * 1.4426950408889634f);
    float ls = e * lz;
    ls += dpp<XOR1>(ls); ls += dpp<XOR2>(ls); ls += dpp<HMIR>(ls);
    const float wz = e * __builtin_amdgcn_rcpf(ls);
#pragma unroll
    for (int kk = 0; kk < 2; ++kk) {
      u32x4 af;
#pragma unroll
      for (int u = 0; u < 4; ++u) af[u] = pack2(gval(ga[kk * 4 + u].x) * wz, gval(ga[kk * 4 + u].z) * wz);
      *(LAS u32x4*)(L + L_A + kk * 1024 + lane * 16) = af;
    }
    asm volatile("s_waitcnt lgkmcnt(0)" ::: "memory");
    *(volatile LAS u32*)(L + L_FA) = 1u;
    return;
  } else {
    stage_wt();
    if (wave > 5) return;
    lds_wait(L + L_CW, 8);
    u32x4 bf[4];
#pragma unroll
    for (int ks = 0; ks < 4; ++ks) bf[ks] = bfrag(L + L_WO, lane, 32 * ks, 16 * t);
    float yb = 0.f;
    if (mlpp) {
      const u32x4 ha0 = *(LAS u32x4*)(L + L_HID + g4 * 16), ha1 = *(LAS u32x4*)(L + L_HID + 64 + g4 * 16);
      f32x4 d = {0.f, 0.f, 0.f, 0.f};
      d = mfma(ha0, bfrag(L + L_W2, lane, 0, 16 * t), d);
      d = mfma(ha1, bfrag(L + L_W2, lane, 32, 16 * t), d);
      yb = d[0];
    }
    asm volatile("" : "+v"(yb), "+v"(bf[0]), "+v"(bf[1]), "+v"(bf[2]), "+v"(bf[3]));
    lds_wait(L + L_FA, 1);
    const u32x4 a0 = *(LAS u32x4*)(L + L_A + lane * 16), a1 = *(LAS u32x4*)(L + L_A + 1024 + lane * 16);
    f32x4 d1 = {0.f, 0.f, 0.f, 0.f}, d2 = {0.f, 0.f, 0.f, 0.f};
    d1 = mfma(a0, bf[0], d1); d2 = mfma(a0, bf[2], d2);
    d1 = mfma(a1, bf[1], d1); d2 = mfma(a1, bf[3], d2);
    const float u1 = (d1[0] + d1[1]) + (d1[2] + d1[3]), u2 = (d2[0] + d2[1]) + (d2[2] + d2[3]);
    const float y = xrow_sum(lane < 32 ? u1 : u2) + yb;
    if (lane < 16) __builtin_amdgcn_raw_buffer_store_b64(u32x2{gword(y, tag), tag.hi}, rws, WS_X3 + (((sp * 5 + t) * 32 + hp) * 16 + lane) * 8, 0, AUX_SC1);
    if (hp != t) return;
  }
  {
    const int x3off = WS_X3 + (((sp * 5 + t) * 32 + (lane >> 3)) * 16 + 2 * (lane & 7)) * 8;
    u32x4 gy[4];
    asm volatile("s_waitcnt vmcnt(0)" ::: "memory");
    __builtin_amdgcn_s_sleep(8);
#pragma unroll
    for (int i = 0; i < 4; ++i) gy[i] = __builtin_amdgcn_raw_buffer_load_b128(rws, x3off + i * 1024, 0, AUX_SC1);
    for (u32 spin = 0; spin < SPIN_CAP; ++spin) {
      u32 bad = 0;
#pragma unroll
      for (int i = 0; i < 4; ++i) bad |= gbad(gy[i], tag);
      if (__builtin_amdgcn_ballot_w64(bad != 0) == 0) break;
      asm volatile("" ::: "memory");
      if (bad != 0) {
#pragma unroll
        for (int i = 0; i < 4; ++i) gy[i] = __builtin_amdgcn_raw_buffer_load_b128(rws, x3off + i * 1024, 0, AUX_SC1);
      }
    }
    float y0 = (gval(gy[0].x) + gval(gy[1].x)) + (gval(gy[2].x) + gval(gy[3].x));
    float y1 = (gval(gy[0].z) + gval(gy[1].z)) + (gval(gy[2].z) + gval(gy[3].z));
    y0 += dpp<ROR8>(y0); y1 += dpp<ROR8>(y1);
    y0 = xrow_sum(y0); y1 = xrow_sum(y1);
    if (lane < 8)
      __builtin_amdgcn_raw_buffer_store_b32(pack2(lo16(xo) + y0, hi16(xo) + y1), __builtin_amdgcn_make_buffer_rsrc((void*)a.out, 0, D * 2, RSRC_W3),
                                            (40 * sp + 8 * t + lane) * 4, 0, AUX_SC1);
    if (hp == 0 && sp == 0 && lane == 0) __hip_atomic_store((GAS u64*)(a.ws + WS_EPOCH), epoch, __ATOMIC_RELAXED, __HIP_MEMORY_SCOPE_AGENT);
  }
}

}

extern "C" void kernel_launch(void* const* d_in, const int* in_sizes, int n_in, void* d_out, int out_size, void* d_ws, size_t ws_size, hipStream_t stream) {
  fused::Args a;
  a.x = (const unsigned short*)d_in[0];
  a.K = (const unsigned short*)d_in[1];
  a.V = (const unsigned short*)d_in[2];
  a.wq = (const unsigned short*)d_in[3];
  a.wo = (const unsigned short*)d_in[4];
  a.w1 = (const unsigned short*)d_in[6];
  a.w3 = (const unsigned short*)d_in[7];
  a.w2 = (const unsigned short*)d_in[8];
  a.out = (unsigned short*)d_out;
  a.ws = (unsigned char*)d_ws;
  hipLaunchKernelGGL(fused::fused_kernel, dim3(fused::NWG), dim3(fused::NT), fused::L_END, stream, a);
}
```
